```python
import torch

def _softmax(x, axis=-1):
    return torch.softmax(x, dim=axis)
def _gelu(x):
    return torch.nn.functional.gelu(x, approximate="tanh")
def _silu(x):
    return torch.nn.functional.silu(x)
def _sigmoid(x):
    return torch.sigmoid(x)
def _layernorm(x, axis=-1, eps=1e-5):
    m = x.mean(dim=axis, keepdim=True)
    v = x.var(dim=axis, keepdim=True, unbiased=False)
    return (x - m) / torch.sqrt(v + eps)
def _scatter_add(tgt, idx, src, axis=0):
    return tgt.clone().index_add_(axis, idx, src)
def _segment_sum(src, seg, n):
    out = torch.zeros((n,) + src.shape[1:], dtype=src.dtype)
    return out.index_add_(0, seg, src)
def _pad(x, pw):
    flat = tuple(v for pair in reversed(pw) for v in pair)
    return torch.nn.functional.pad(x, flat)

kernel_name = 'recurrent_scan__ssm_conv_s4d__dense_activation_relu__prompt'

def setup_inputs():

    _w = (lambda a: (lambda t: t.to(torch.bfloat16) if t.is_floating_point() else t)(torch.as_tensor(a)))
    def _A_setup():
        Bsz, L, D, N = 1, 2048, 448, 256
        return {
            "u": _w((lambda *s: torch.randn(*s, dtype=torch.bfloat16) * 0.05)(Bsz, L, D)),
            "log_A_real": _w((lambda *s: torch.randn(*s, dtype=torch.bfloat16) * 6.4)(D, N) * 0.5),
            "A_imag": _w((lambda *s: torch.randn(*s, dtype=torch.bfloat16) * 0.05)(D, N) * 3.14),
            "C_real": _w((lambda *s: torch.randn(*s, dtype=torch.bfloat16) * 0.05)(D, N) / N ** 0.5),
            "C_imag": _w((lambda *s: torch.randn(*s, dtype=torch.bfloat16) * 0.05)(D, N) / N ** 0.5),
            "log_dt": _w((lambda *s: torch.randn(*s, dtype=torch.bfloat16) * 0.05)(D) * 0.5 - 4.0),
            "D_skip": _w((lambda *s: torch.randn(*s, dtype=torch.bfloat16) * 0.05)(D)),
        }
    _A_in = _A_setup()

    def _B_setup():
        B, S, D, Dff = 1, 2048, 448, 1792
        return {
            "x": _w((lambda *s: torch.randn(*s, dtype=torch.bfloat16) * 0.05)(B, S, D)),
            "w1": _w((lambda *s: torch.randn(*s, dtype=torch.bfloat16) * 0.05)(D, Dff)),
            "w2": _w((lambda *s: torch.randn(*s, dtype=torch.bfloat16) * 0.05)(Dff, D)),
        }
    _B_in = _B_setup()

    return {
        "A_u": _A_in["u"],
        "A_log_A_real": _A_in["log_A_real"],
        "A_A_imag": _A_in["A_imag"],
        "A_C_real": _A_in["C_real"],
        "A_C_imag": _A_in["C_imag"],
        "A_log_dt": _A_in["log_dt"],
        "A_D_skip": _A_in["D_skip"],
        "B_x": _B_in["x"],
        "B_w1": _B_in["w1"],
        "B_w2": _B_in["w2"],
    }

def reference(A_u, A_log_A_real, A_A_imag, A_C_real, A_C_imag, A_log_dt, A_D_skip, B_x, B_w1, B_w2):
    def _A_ref(u, log_A_real, A_imag, C_real, C_imag, log_dt, D_skip):
        L = u.shape[1]
        dt = torch.exp(log_dt)
        A = -torch.exp(log_A_real) + 1j * A_imag
        C = (C_real + 1j * C_imag) * dt[:, None]
        pos = (lambda x, dt: x.to(dt))(torch.arange(L), log_dt.dtype)
        P = torch.exp((A * dt[:, None])[:, :, None] * pos[None, None, :])
        K = 2.0 * torch.einsum("dn,dnl->dl", C, P).real
        L2 = 2 * L
        u_t = (lambda x, *d: x.permute(*d))(u, 0, 2, 1)
        Kf = (lambda x, *a, **k: torch.fft.rfft(x.float(), *a, **k))(K, L2)
        Uf = (lambda x, *a, **k: torch.fft.rfft(x.float(), *a, **k))(u_t, L2)
        y = (lambda x, *a, **k: torch.fft.irfft(x, *a, **k).to(torch.bfloat16))(Uf * Kf[None, :, :], L2)[:, :, :L]
        y = (lambda x, *d: x.permute(*d))(y, 0, 2, 1)
        return y + u * D_skip[None, None, :]

    def _B_ref(x, w1, w2):
        h = torch.einsum("bsd,df->bsf", x, w1)
        zero = torch.zeros_like(h)
        h = torch.maximum(h, zero)
        return torch.einsum("bsf,fd->bsd", h, w2)

    x = A_u
    xn = _layernorm(x, axis=-1)
    s = _A_ref(xn, A_log_A_real, A_A_imag, A_C_real, A_C_imag, A_log_dt, A_D_skip)
    g = _silu(_B_ref(xn, B_w1, B_w2))
    return x + s * g
```

```cpp
#include <hip/hip_runtime.h>
#include <hip/hip_bf16.h>
#include <stdint.h>

constexpr int L = 2048, D = 448, NS = 256, FF = 1792;
typedef unsigned short u16;

__device__ __forceinline__ float bf2f(u16 v) { return __uint_as_float(((unsigned)v) << 16); }
__device__ __forceinline__ u16 f2bf(float f) { __hip_bfloat16 b = __float2bfloat16(f); return *reinterpret_cast<u16*>(&b); }
__device__ __forceinline__ float rbf(float f) { return bf2f(f2bf(f)); }

__global__ void k_ln(const u16* x, u16* xn) {
  int row = blockIdx.x, t = threadIdx.x;
  float v[7]; float s = 0.f;
  for (int i = 0; i < 7; ++i) { v[i] = bf2f(x[row * D + t + 64 * i]); s += v[i]; }
  for (int o = 32; o; o >>= 1) s += __shfl_xor(s, o);
  float mean = s / D; float q = 0.f;
  for (int i = 0; i < 7; ++i) { float d = v[i] - mean; q += d * d; }
  for (int o = 32; o; o >>= 1) q += __shfl_xor(q, o);
  float m = rbf(mean), var = rbf(q / D);
  float sd = rbf(sqrtf(rbf(var + 1e-5f)));
  for (int i = 0; i < 7; ++i) xn[row * D + t + 64 * i] = f2bf(rbf(v[i] - m) / sd);
}

__global__ void k_kgen(const u16* logA, const u16* Aim, const u16* Cr, const u16* Ci, const u16* logdt, float* K) {
  int d = blockIdx.x;
  __shared__ float zr[NS], zi[NS], cr[NS], ci[NS];
  float dt = rbf(expf(bf2f(logdt[d])));
  for (int n = threadIdx.x; n < NS; n += blockDim.x) {
    float ar = -rbf(expf(bf2f(logA[d * NS + n])));
    zr[n] = ar * dt; zi[n] = bf2f(Aim[d * NS + n]) * dt;
    cr[n] = bf2f(Cr[d * NS + n]) * dt; ci[n] = bf2f(Ci[d * NS + n]) * dt;
  }
  __syncthreads();
  for (int l = threadIdx.x; l < L; l += blockDim.x) {
    float p = rbf((float)l);
    float acc = 0.f;
    for (int n = 0; n < NS; ++n) {
      float e = expf(zr[n] * p); float s, c; sincosf(zi[n] * p, &s, &c);
      acc += cr[n] * (e * c) - ci[n] * (e * s);
    }
    K[d * L + l] = 2.f * acc;
  }
}

__global__ void k_conv(const u16* xn, const float* K, const u16* Dskip, u16* s) {
  int d = blockIdx.x;
  __shared__ float u[L], k[L];
  for (int l = threadIdx.x; l < L; l += blockDim.x) { u[l] = bf2f(xn[l * D + d]); k[l] = K[d * L + l]; }
  __syncthreads();
  float ds = bf2f(Dskip[d]);
  for (int l = threadIdx.x; l < L; l += blockDim.x) {
    float acc = 0.f;
    for (int j = 0; j <= l; ++j) acc += k[j] * u[l - j];
    s[l * D + d] = f2bf(rbf(acc) + rbf(u[l] * ds));
  }
}

__global__ void k_gemm1(const u16* xn, const u16* w1, u16* h) {
  int row = blockIdx.x;
  __shared__ float xs[D];
  for (int i = threadIdx.x; i < D; i += blockDim.x) xs[i] = bf2f(xn[row * D + i]);
  __syncthreads();
  for (int f = threadIdx.x; f < FF; f += blockDim.x) {
    float acc = 0.f;
    for (int k = 0; k < D; ++k) acc += xs[k] * bf2f(w1[k * FF + f]);
    h[row * FF + f] = f2bf(fmaxf(rbf(acc), 0.f));
  }
}

__global__ void k_gemm2(const u16* h, const u16* w2, const u16* s, const u16* x, u16* out) {
  int row = blockIdx.x;
  __shared__ float hs[FF];
  for (int i = threadIdx.x; i < FF; i += blockDim.x) hs[i] = bf2f(h[row * FF + i]);
  __syncthreads();
  for (int c = threadIdx.x; c < D; c += blockDim.x) {
    float acc = 0.f;
    for (int k = 0; k < FF; ++k) acc += hs[k] * bf2f(w2[k * D + c]);
    float o = rbf(acc);
    float g = rbf(o / (1.f + expf(-o)));
    float sg = rbf(bf2f(s[row * D + c]) * g);
    out[row * D + c] = f2bf(bf2f(x[row * D + c]) + sg);
  }
}

extern "C" void kernel_launch(void* const* d_in, const int* in_sizes, int n_in,
                              void* d_out, int out_size, void* d_ws, size_t ws_size,
                              hipStream_t stream) {
  const u16* x = (const u16*)d_in[0];
  const u16* logA = (const u16*)d_in[1];
  const u16* Aim = (const u16*)d_in[2];
  const u16* Cr = (const u16*)d_in[3];
  const u16* Ci = (const u16*)d_in[4];
  const u16* logdt = (const u16*)d_in[5];
  const u16* Dskip = (const u16*)d_in[6];
  const u16* w1 = (const u16*)d_in[8];
  const u16* w2 = (const u16*)d_in[9];
  char* ws = (char*)d_ws;
  u16* xn = (u16*)ws;
  float* K = (float*)(ws + (4 << 20));
  u16* s = (u16*)(ws + (8 << 20));
  u16* h = (u16*)(ws + (12 << 20));
  k_ln<<<L, 64, 0, stream>>>(x, xn);
  k_kgen<<<D, 256, 0, stream>>>(logA, Aim, Cr, Ci, logdt, K);
  k_conv<<<D, 256, 0, stream>>>(xn, K, Dskip, s);
  k_gemm1<<<L, 256, 0, stream>>>(xn, w1, h);
  k_gemm2<<<L, 256, 0, stream>>>(h, w2, s, x, (u16*)d_out);
}
```

```cpp
#include <hip/hip_runtime.h>
#include <hip/hip_bf16.h>
#include <stdint.h>

namespace {
constexpr int L = 2048, D = 448, NS = 256, FF = 1792;
typedef unsigned short u16;
typedef unsigned int u32;
typedef __attribute__((ext_vector_type(2))) float f32x2;
typedef __attribute__((ext_vector_type(4))) float f32x4;
typedef __attribute__((ext_vector_type(16))) float f32x16;
typedef __attribute__((ext_vector_type(2))) _Float16 h16x2;
typedef __attribute__((ext_vector_type(8))) _Float16 h16x8;
typedef __attribute__((ext_vector_type(2))) __bf16 bf16x2;
typedef __attribute__((ext_vector_type(8))) __bf16 bf16x8;
typedef __attribute__((ext_vector_type(4))) short s16x4;
typedef __attribute__((ext_vector_type(8))) short s16x8;
typedef __attribute__((ext_vector_type(2))) u32 u32x2;
typedef __attribute__((ext_vector_type(4))) u32 u32x4;
#define LAS __attribute__((address_space(3)))
#define DI __device__ __forceinline__

constexpr size_t WS_UT = 0;
constexpr size_t WS_KT = 2u << 20;
constexpr size_t WS_KS = 10u << 20;
constexpr size_t WS_H = 12u << 20;
constexpr size_t WS_ST = 20u << 20;
constexpr size_t WS_FLAG = 24u << 20;
constexpr size_t WS_RINV = 25u << 20;
constexpr size_t WS_W2F = 26u << 20;
constexpr int LP = L + 128;
constexpr int FSP = 16;
constexpr int KT_COPY = 4160;
constexpr int KT_BYTES = 4 * KT_COPY;

DI float bf2f(u16 v) { return __uint_as_float(((u32)v) << 16); }
DI float rbf(float f) { return (float)(__bf16)f; }
DI u32 pk_bf16(float a, float b) { f32x2 v = {a, b}; return __builtin_bit_cast(u32, __builtin_convertvector(v, bf16x2)); }
DI u32 pk_f16(f32x2 v) { return __builtin_bit_cast(u32, __builtin_convertvector(v, h16x2)); }

DI f32x2 cmul(f32x2 a, f32x2 b, f32x2 bs) {
  f32x2 t = a.xx * b;
  return __builtin_elementwise_fma(a.yy, bs, t);
}
DI f32x2 rot(f32x2 b) { f32x2 r = {-b.y, b.x}; return r; }

template <int CTRL> DI int dpp_i(int x) { return __builtin_amdgcn_update_dpp(0, x, CTRL, 0xf, 0xf, false); }
DI float wave_sum(float x) {
  x += __builtin_bit_cast(float, dpp_i<0xB1>(__builtin_bit_cast(int, x)));
  x += __builtin_bit_cast(float, dpp_i<0x4E>(__builtin_bit_cast(int, x)));
  x += __builtin_bit_cast(float, dpp_i<0x141>(__builtin_bit_cast(int, x)));
  x += __builtin_bit_cast(float, dpp_i<0x140>(__builtin_bit_cast(int, x)));
  int xi = __builtin_bit_cast(int, x);
  float a = __builtin_bit_cast(float, __builtin_amdgcn_readlane(xi, 0)), b = __builtin_bit_cast(float, __builtin_amdgcn_readlane(xi, 16));
  float c = __builtin_bit_cast(float, __builtin_amdgcn_readlane(xi, 32)), d = __builtin_bit_cast(float, __builtin_amdgcn_readlane(xi, 48));
  return (a + b) + (c + d);
}
DI int wave_max_i(int x) {
  x = max(x, dpp_i<0xB1>(x)); x = max(x, dpp_i<0x4E>(x)); x = max(x, dpp_i<0x141>(x)); x = max(x, dpp_i<0x140>(x));
  return max(max(__builtin_amdgcn_readlane(x, 0), __builtin_amdgcn_readlane(x, 16)), max(__builtin_amdgcn_readlane(x, 32), __builtin_amdgcn_readlane(x, 48)));
}

DI void glds16(const void* sbase, u32 voff, u32 ldst) {
  u32 keep;
  asm volatile("s_mov_b32 %0, m0\n\ts_mov_b32 m0, %3\n\ts_nop 0\n\tglobal_load_lds_dwordx4 %1, %2\n\ts_mov_b32 m0, %0"
               : "=&s"(keep) : "v"(voff), "s"(sbase), "s"(ldst) : "memory");
}
#define WAIT_VM(n) asm volatile("s_waitcnt vmcnt(" #n ")" ::: "memory")
#define WAIT_LGKM0() asm volatile("s_waitcnt lgkmcnt(0)" ::: "memory")

DI void lds_wait4(LAS char* sm, u32 off) {
  u32 addr = (u32)(size_t)sm + off, tv, td, ts;
  asm volatile("v_mov_b32 %0, %3\n"
               "WL%=:\n\ts_sleep 1\n\tds_read_b32 %1, %0\n\ts_waitcnt lgkmcnt(0)\n\tv_readfirstlane_b32 %2, %1\n\ts_cmp_lg_u32 %2, 0x01010101\n\ts_cbranch_scc1 WL%=\n"
               : "=&v"(tv), "=&v"(td), "=&s"(ts) : "s"(addr) : "memory", "scc");
}
DI u32 flag_pre(LAS char* sm, u32 off) { return *(volatile LAS u32*)(sm + off); }
DI void flag_chk(u32 v, LAS char* sm, u32 off) {
  u32 addr = (u32)(size_t)sm + off, tv, ts;
  asm volatile("v_readfirstlane_b32 %2, %0\n\ts_cmp_eq_u32 %2, 0x01010101\n\ts_cbranch_scc1 FD%=\n\tv_mov_b32 %1, %3\n"
               "FL%=:\n\tds_read_b32 %0, %1\n\ts_waitcnt lgkmcnt(0)\n\tv_readfirstlane_b32 %2, %0\n\ts_cmp_lg_u32 %2, 0x01010101\n\ts_cbranch_scc1 FL%=\nFD%=:\n"
               : "+v"(v), "=&v"(tv), "=&s"(ts) : "s"(addr) : "memory", "scc");
}
DI void lds_set(LAS char* sm, u32 off) {
  *(volatile LAS unsigned char*)(sm + off) = 1;
}
DI void lds_bar4(LAS char* sm, u32 off, u32 target, int lane) {
  WAIT_LGKM0();
  if (lane == 0) __hip_atomic_fetch_add((LAS u32*)(sm + off), 1u, __ATOMIC_RELAXED, __HIP_MEMORY_SCOPE_WORKGROUP);
  for (;;) {
    u32 v = *(volatile LAS u32*)(sm + off);
    if ((u32)__builtin_amdgcn_readfirstlane(v) >= target) break;
  }
  asm volatile("" ::: "memory");
}

#define STAMP(i)
#define STAMP_DUMP()
#define RSTAMP(i)
#define DBG_ARG
#define DBG_PASS
#define DBG_PASS1
#define DBG_PASS2
constexpr u32 G1_W = 0;
constexpr u32 G1_X = 114688;
constexpr u32 KG_ROW = 272, KG_SZ = 53 * KG_ROW;
constexpr u32 LN_T = 147456;
constexpr u32 K2_S = 131072;
constexpr u32 G1_CS = 159904;
constexpr u32 G1_RS = G1_CS + 1024;
constexpr u32 K1_FL = G1_RS + 1024;
constexpr u32 P_LDS = K1_FL + 256;
constexpr u32 K2_RED = 0;
constexpr u32 K2_PT = 110592;
constexpr u32 CV_U = 98304;
constexpr u32 CV_K = 110592;
constexpr u32 CV_P = 110592;
constexpr u32 K2_T = 151552;
constexpr int UG = 7424;
#define CBAR() do { asm volatile("" ::: "memory"); __builtin_amdgcn_s_barrier(); asm volatile("" ::: "memory"); } while (0)
typedef __attribute__((address_space(1))) u32 gu32;
#define RLX_AGENT __ATOMIC_RELAXED, __HIP_MEMORY_SCOPE_AGENT
constexpr int FL_H = 1024 * FSP, FL_U = 4096 * FSP, FL_S = 6144 * FSP;
#define PSLEEP 8
DI u32 epoch_load(const u32* flags) { return __hip_atomic_load((const gu32*)flags, RLX_AGENT); }
#define TAG_READY(raw, tag) do { asm volatile("" : "+v"(raw)); tag = raw + 1u; } while (0)
DI void poll_flags(const u32* p, u32 tag) {
  for (u32 spins = 0; spins < (1u << 22); ++spins) {
    u32 v = __hip_atomic_load((const gu32*)p, RLX_AGENT);
    if (__all(v == tag)) break;
    __builtin_amdgcn_s_sleep(PSLEEP);
  }
  asm volatile("" ::: "memory");
}

struct LutT { u16 v[2][256][12]; };
constexpr LutT make_lut() {
  LutT t{};
  for (int ch = 0; ch < 2; ++ch)
    for (int th = 0; th < 256; ++th)
      for (int e = 0; e < 12; ++e) {
        int j = 2047 - 8 * th - e;
        int off = 22 * 128;
        if (j >= 0) {
          int p = j;
          if (j >= 256) { int sh = j >= 1024 ? 3 : (j >= 512 ? 2 : 1); int q = j >> sh, r = j & ((1 << sh) - 1), hf = 1 << (sh - 1); if (r > hf || (r == hf && (q & 1))) ++q; p = q << sh; }
          int b = (p & 255) >> 3, lo = p & 7, k = p >> 8, a = 0;
          if (k == 0) a = lo; else if (lo == 0) a = 10 + k; else if (k == 1) a = 7 + (lo >> 1); else a = 17 + k;
          off = (a * 32 + b) * 4;
        }
        t.v[ch][th][e] = (u16)(off + ch * 2944);
      }
  return t;
}
__device__ const LutT g_lut = make_lut();

DI f32x2 cexp_hw(float zr, float zi, float p) {
  float ph = zi * p;
  float k = __builtin_rintf(ph * 0.15915494309189535f);
  float r = __builtin_fmaf(ph, 0.15915494309189535f, -k);
  r = __builtin_fmaf(ph, 6.4206382433e-09f, r);
  float e = __builtin_amdgcn_exp2f(zr * p * 1.4426950408889634f);
  return (f32x2){e * __builtin_amdgcn_cosf(r), e * __builtin_amdgcn_sinf(r)};
}
DI void lds_bar8(LAS char* sm, u32 off, int kw) {
  WAIT_LGKM0();
  *(volatile LAS unsigned char*)(sm + off + kw) = 1;
  u32 addr = (u32)(size_t)sm + off, tv, td, te, ts;
  asm volatile("v_mov_b32 %0, %4\n"
               "WB%=:\n\ts_sleep 1\n\tds_read_b32 %1, %0\n\tds_read_b32 %2, %0 offset:4\n\ts_waitcnt lgkmcnt(0)\n\tv_and_b32 %1, %1, %2\n\ts_nop 0\n\tv_readfirstlane_b32 %3, %1\n\ts_cmp_lg_u32 %3, 0x01010101\n\ts_cbranch_scc1 WB%=\n"
               : "=&v"(tv), "=&v"(td), "=&v"(te), "=&s"(ts) : "s"(addr) : "memory", "scc");
}
DI void lds_bar8s(LAS char* sm, u32 off, int kw) {
  WAIT_LGKM0();
  *(volatile LAS unsigned char*)(sm + off + kw) = 1;
  u32 addr = (u32)(size_t)sm + off, tv, td, te, ts;
  asm volatile("v_mov_b32 %0, %4\n"
               "WS%=:\n\ts_sleep 2\n\tds_read_b32 %1, %0\n\tds_read_b32 %2, %0 offset:4\n\ts_waitcnt lgkmcnt(0)\n\tv_and_b32 %1, %1, %2\n\ts_nop 0\n\tv_readfirstlane_b32 %3, %1\n\ts_cmp_lg_u32 %3, 0x01010101\n\ts_cbranch_scc1 WS%=\n"
               : "=&v"(tv), "=&v"(td), "=&v"(te), "=&s"(ts) : "s"(addr) : "memory", "scc");
}
struct cf { float r, i; };
DI float amul(float a, float b) { float o; asm("v_mul_f32 %0, %1, %2" : "=v"(o) : "v"(a), "v"(b)); return o; }
DI void afmac(float& d, float a, float b) { asm("v_fmac_f32 %0, %1, %2" : "+v"(d) : "v"(a), "v"(b)); }
struct cm { float r, i, ni; };
DI cm mk(cf e) { return cm{e.r, e.i, -e.i}; }
DI cf cmul1(cf a, cm b) { cf o; o.r = amul(a.r, b.r); o.i = amul(a.r, b.i); afmac(o.r, a.i, b.ni); afmac(o.i, a.i, b.r); return o; }
DI u32 pkc(cf a) { return pk_f16((f32x2){a.r, a.i}); }
DI cf cexp1(float zr, float zi, float p) { f32x2 e = cexp_hw(zr, zi, p); return cf{e.x, e.y}; }

DI void kgen_scales(float dt, u32x2 c4r, u32x2 c4i, float& SG, float& F, float& invKS) {
  typedef __attribute__((ext_vector_type(2))) unsigned short u16x2;
  u16x2 m = __builtin_elementwise_max(__builtin_elementwise_max(__builtin_bit_cast(u16x2, c4r.x & 0x7fff7fffu), __builtin_bit_cast(u16x2, c4r.y & 0x7fff7fffu)),
                                      __builtin_elementwise_max(__builtin_bit_cast(u16x2, c4i.x & 0x7fff7fffu), __builtin_bit_cast(u16x2, c4i.y & 0x7fff7fffu)));
  float cmax = __uint_as_float((u32)wave_max_i((int)max((u32)m.x, (u32)m.y)) << 16) * dt;
  int ec = min(max((int)(__float_as_uint(cmax) >> 23), 14), 240);
  SG = __uint_as_float((u32)(267 - ec) << 23);
  F = __uint_as_float((u32)(127 - 23) << 23);
  invKS = __uint_as_float((u32)(ec - 3) << 23);
}
DI float exp_bf(float x) {
  float t = x * 1.4426950408889634f;
  float lo = __builtin_fmaf(x, 1.4426950408889634f, -t);
  lo = __builtin_fmaf(x, 1.925963033500e-08f, lo);
  float e = __builtin_amdgcn_exp2f(t);
  return __builtin_fmaf(e * lo, 0.6931471805599453f, e);
}
DI u32 hmul(u32 a, u32 m, u32 mr) {
  h16x2 av = __builtin_bit_cast(h16x2, a);
  h16x2 t = av.xx * __builtin_bit_cast(h16x2, m);
  return __builtin_bit_cast(u32, __builtin_elementwise_fma(av.yy, __builtin_bit_cast(h16x2, mr), t));
}
DI u32 pkr(cf a) { return pk_f16((f32x2){-a.i, a.r}); }

DI void ln_row(u32x4 xv, int lane, float (&nv)[8], u32x2& stat) {
  float v[8];
#pragma unroll
  for (int i = 0; i < 4; ++i) { v[2 * i] = __uint_as_float(xv[i] << 16); v[2 * i + 1] = __uint_as_float(xv[i] & 0xffff0000u); }
  float sm1 = ((v[0] + v[1]) + (v[2] + v[3])) + ((v[4] + v[5]) + (v[6] + v[7]));
  sm1 = lane < 56 ? sm1 : 0.f;
  const float mean = wave_sum(sm1) * (1.f / D);
  float sq = 0.f;
#pragma unroll
  for (int i = 0; i < 8; ++i) { float dd = v[i] - mean; sq = __builtin_fmaf(dd, dd, sq); }
  sq = lane < 56 ? sq : 0.f;
  const float var = wave_sum(sq) * (1.f / D);
  const float mb = rbf(mean), sd = rbf(__builtin_amdgcn_sqrtf(rbf(rbf(var) + 1e-5f))), rinv = __builtin_amdgcn_rcpf(sd);
#pragma unroll
  for (int i = 0; i < 8; ++i) { float dd = rbf(v[i] - mb); float qv = dd * rinv; float rr = __builtin_fmaf(-qv, sd, dd); nv[i] = rbf(__builtin_fmaf(rr, rinv, qv)); }
  stat = (u32x2){pk_bf16(-mb, -mb), __float_as_uint(rinv)};
}
DI void ln_group_row(LAS char* sm, u32x4 xrow, int lane, int r, int g, char* UT, char* RST) {
  const __amdgpu_buffer_rsrc_t srs = __builtin_amdgcn_make_buffer_rsrc(RST, 0, L * 8, 0x00020000);
  float nv[8]; u32x2 st;
  ln_row(xrow, lane, nv, st);
  if (lane == 0) __builtin_amdgcn_raw_buffer_store_b64(st, srs, (u32)((g * 8 + r) * 8), 0, 16);
  if (lane < 56) {
#pragma unroll
    for (int i = 0; i < 8; ++i) *(LAS u16*)(sm + LN_T + (8 * lane + (i ^ (lane & 7))) * 16 + r * 2) = __builtin_bit_cast(u16, (_Float16)nv[i]);
  }
  lds_bar8(sm, K1_FL + 32, r);
  if (r < 7) {
    const __amdgpu_buffer_rsrc_t urs = __builtin_amdgcn_make_buffer_rsrc(UT, 0, 256 * UG, 0x00020000);
    const int e = r * 64 + lane;
    u32x4 ov = *(LAS u32x4*)(sm + LN_T + (e ^ ((e >> 3) & 7)) * 16);
    __builtin_amdgcn_raw_buffer_store_b128(ov, urs, (u32)(g * UG + e * 16), 0, 16);
  }
}
DI void ln_publish(LAS char* sm, int lane, u32* fl, u32 tag) {
  u32 old = 0;
  if (lane == 0) old = __hip_atomic_fetch_add((LAS u32*)(sm + K1_FL + 52), 1u, __ATOMIC_RELAXED, __HIP_MEMORY_SCOPE_WORKGROUP);
  if (lane == 0 && old == 7) __hip_atomic_store((gu32*)fl, tag, RLX_AGENT);
}

DI void m_role(LAS char* sm, int wv, int lane, int pm, int pn, u32& tag, const u16* x, const u16* w1, char* UT, char* RST, char* HF, const u16* w2, u32* flags DBG_ARG) {
  STAMP(0);
  const int r32 = lane & 31, h = lane >> 5;
  LAS char* xr = sm + G1_X + wv * 4096 + r32 * 128;
  const int xsw = (r32 >> 1) & 7;
  LAS char* xrs[4];
#pragma unroll
  for (int s = 0; s < 4; ++s) xrs[s] = xr + (((2 * s + h) ^ xsw) << 4);
  const int q = (lane & 15) >> 2, p = lane & 3, blk = (lane >> 4) & 1;
  LAS char* wbs[4];
#pragma unroll
  for (int c = 0; c < 4; ++c) wbs[c] = sm + G1_W + (8 * h + q) * 256 + ((((c + wv) & 3) ^ q) << 6) + blk * 32 + p * 8;
  struct Frag { s16x4 a[4][2]; };
  auto rd = [&](int s, Frag& f) {
#pragma unroll
    for (int c = 0; c < 4; ++c) {
      f.a[c][0] = __builtin_amdgcn_ds_read_tr16_b64_v4i16((LAS s16x4*)(wbs[c] + s * 4096));
      f.a[c][1] = __builtin_amdgcn_ds_read_tr16_b64_v4i16((LAS s16x4*)(wbs[c] + s * 4096 + 1024));
    }
  };
  f32x16 acc[4] = {};
  f32x16 csa = {};
  const s16x8 ones = {0x3f80, 0x3f80, 0x3f80, 0x3f80, 0x3f80, 0x3f80, 0x3f80, 0x3f80};
  Frag fA, fB;
  const u32 smb1 = (u32)(size_t)sm;
  u32 xoff[4];
#pragma unroll
  for (int i = 0; i < 4; ++i) { int r = 8 * i + (lane >> 3); xoff[i] = (u32)((pm * 128 + wv * 32 + r) * (D * 2) + (((lane & 7) ^ ((r >> 1) & 7)) << 4)); }
  auto xdma = [&](int st, int i) { glds16((const char*)x + st * 128, xoff[i], smb1 + G1_X + (st & 1) * 16384 + wv * 4096 + i * 1024); };
  u32 raw = epoch_load(flags);
  u32x4 xrow;
  { const u32 xo = (u32)(((pm * 16 + pn) * 8 + 6 + (wv & 1)) * (D * 2) + min(lane, 55) * 16);
    asm volatile("global_load_dwordx4 %0, %1, %2" : "=&v"(xrow) : "v"(xo), "s"(x) : "memory"); }
#pragma unroll
  for (int i = 0; i < 4; ++i) xdma(0, i);
  WAIT_LGKM0(); CBAR();
#pragma unroll
  for (int i = 0; i < 4; ++i) xdma(1, i);
  asm volatile("s_waitcnt vmcnt(8)" : "+v"(xrow) :: "memory");
  if (wv < 2) ln_group_row(sm, xrow, lane, 6 + wv, pm * 16 + pn, UT, RST);
  STAMP(1);
  s16x8 xs[2][4];
  auto xrd = [&](int st) {
#pragma unroll
    for (int s4 = 0; s4 < 4; ++s4) xs[st & 1][s4] = *(LAS s16x8*)(xrs[s4] + (st & 1) * 16384);
  };
  WAIT_VM(4);
  xrd(0);
  WAIT_LGKM0();
#pragma unroll
  for (int i = 0; i < 4; ++i) xdma(2, i);
  lds_wait4(sm, K1_FL + 128);
  STAMP(2);
  rd(0, fA);
  u32 pre = 0;
  auto step = [&](int s, Frag& cur, Frag& nxt) {
    const int st = s >> 2, s4 = s & 3;
    if (s4 == 0 && st >= 1) lds_set(sm, K1_FL + 192 + 4 * (st - 1) + wv);
    if (s4 == 2 && st < 6) {
      if (st == 1) { WAIT_VM(4); TAG_READY(raw, tag); if (wv < 2) ln_publish(sm, lane, flags + FL_U + (pm * 16 + pn) * FSP, tag); }
      else if (st <= 4) WAIT_VM(4); else WAIT_VM(0);
      xrd(st + 1);
      pre = flag_pre(sm, K1_FL + 128 + 4 * (st + 1));
    }
    if (s4 == 3 && st < 6) flag_chk(pre, sm, K1_FL + 128 + 4 * (st + 1));
    if (s < 27) rd(s + 1, nxt);
    bf16x8 bx = __builtin_bit_cast(bf16x8, xs[st & 1][s4]);
#define MM(c) acc[c] = __builtin_amdgcn_mfma_f32_32x32x16_bf16(__builtin_bit_cast(bf16x8, __builtin_shufflevector(cur.a[c][0], cur.a[c][1], 0, 1, 2, 3, 4, 5, 6, 7)), bx, acc[c], 0, 0, 0)
    MM(0); MM(1); MM(2);
    __builtin_amdgcn_sched_group_barrier(0x8, 1, 0); __builtin_amdgcn_sched_group_barrier(0x100, 4, 0);
    __builtin_amdgcn_sched_group_barrier(0x8, 1, 0); __builtin_amdgcn_sched_group_barrier(0x100, 4, 0);
    __builtin_amdgcn_sched_group_barrier(0x8, 1, 0); __builtin_amdgcn_sched_group_barrier(0x100, 4, 0);
    __builtin_amdgcn_sched_barrier(0);
    if (s4 == 3 && st + 3 <= 6) { WAIT_LGKM0(); xdma(st + 3, 0); }
    if (s4 == 0 && st >= 1 && st + 2 <= 6) xdma(st + 2, 2);
    MM(3);
    __builtin_amdgcn_sched_barrier(0);
    if (s4 == 3 && st + 3 <= 6) xdma(st + 3, 1);
    if (s4 == 0 && st >= 1 && st + 2 <= 6) xdma(st + 2, 3);
    csa = __builtin_amdgcn_mfma_f32_32x32x16_bf16(__builtin_bit_cast(bf16x8, ones), __builtin_bit_cast(bf16x8, __builtin_shufflevector(cur.a[0][0], cur.a[0][1], 0, 1, 2, 3, 4, 5, 6, 7)), csa, 0, 0, 0);
#undef MM
    __builtin_amdgcn_sched_barrier(0);
  };
#pragma unroll
  for (int s = 0; s < 28; s += 2) {
    if ((s & 3) == 0) STAMP(5 + (s >> 2));
    step(s, fA, fB);
    step(s + 1, fB, fA);
  }
  STAMP(12);
  {
    float c0 = csa[0];
    float p0 = rbf(c0), r1 = c0 - p0, p1 = rbf(r1), p2 = r1 - p1;
    if (h == 0) *(LAS u32x2*)(sm + G1_CS + (32 * wv + r32) * 8) = (u32x2){pk_bf16(p0, p1), pk_bf16(p2, 0.f)};
  }
  WAIT_LGKM0();
  lds_set(sm, K1_FL + 180 + wv);
  lds_wait4(sm, K1_FL + 180); lds_wait4(sm, K1_FL + 176);
  STAMP(13);
  {
    u32 nmb = *(LAS u32*)(sm + G1_RS + (32 * wv + r32) * 8);
    u32x2 cp[4];
#pragma unroll
    for (int c = 0; c < 4; ++c) cp[c] = *(LAS u32x2*)(sm + G1_CS + (32 * ((c + wv) & 3) + r32) * 8);
    nmb = h ? 0u : nmb;
    u32x4 bm = {nmb, nmb, 0u, 0u};
#pragma unroll
    for (int c = 0; c < 4; ++c) {
      u32x4 am = {cp[c].x, cp[c].y, 0u, 0u};
      acc[c] = __builtin_amdgcn_mfma_f32_32x32x16_bf16(__builtin_bit_cast(bf16x8, am), __builtin_bit_cast(bf16x8, bm), acc[c], 0, 0, 0);
    }
    const __amdgpu_buffer_rsrc_t hrs = __builtin_amdgcn_make_buffer_rsrc(HF, 0, L * FF * 2, 0x00020000);
    const u32 ho = (u32)(((pm * 4 + wv) * 112 + pn * 8) * 1024 + lane * 16);
#pragma unroll
    for (int c = 0; c < 4; ++c) {
      const int ct = (c + wv) & 3;
#pragma unroll
      for (int s2 = 0; s2 < 2; ++s2) {
        typedef __attribute__((ext_vector_type(2))) short s16x2;
        const s16x2 zz = {0, 0};
        u32x4 o;
#pragma unroll
        for (int i = 0; i < 4; ++i)
          o[i] = __builtin_bit_cast(u32, __builtin_elementwise_max(__builtin_bit_cast(s16x2, pk_bf16(acc[c][8 * s2 + 2 * i], acc[c][8 * s2 + 2 * i + 1])), zz));
        __builtin_amdgcn_raw_buffer_store_b128(o, hrs, ho + (2 * ct + s2) * 1024, 0, 16);
      }
    }
  }
  STAMP(14);
  const int pm2 = 2 * pm + (pn >= 7), pc = pn >= 7 ? pn - 7 : pn;
  const u32 smA = (u32)(size_t)sm + wv * 8192, smB = (u32)(size_t)sm + 32768 + wv * 16384 - 8192;
  const u32 woff = (u32)((wv * 448 + (lane >> 3)) * (D * 2) + pc * 128 + (((lane & 7) ^ (((lane >> 4) & 1) << 2)) << 4));
  auto wdma = [&](int t) {
#pragma unroll
    for (int i = 0; i < 4; ++i) glds16((const char*)w2 + (t * 32 + i * 8) * (D * 2), woff, ((t % 6) < 2 ? smA : smB) + (t % 6) * 4096 + i * 1024);
  };
  WAIT_VM(0);
  if (lane == 0) __hip_atomic_store((gu32*)(flags + FL_H + ((pm * 4 + wv) * 16 + pn) * FSP), tag, RLX_AGENT);
  lds_wait4(sm, K1_FL + 64); lds_wait4(sm, K1_FL + 68);
  wdma(0); wdma(1); wdma(2); wdma(3); wdma(4); wdma(5);
  STAMP(15);
  poll_flags(flags + FL_H + ((2 * pm2 + (lane & 1)) * 16 + ((7 * wv) >> 1) + ((lane >> 1) & 3)) * FSP, tag);
  STAMP(16);
  constexpr int PH = 3, NB = PH + 1;
  const char* hb = HF + ((size_t)(pm2 * 2) * 112 + wv * 28) * 1024;
  const u32 lo = lane * 16;
  u32x4 hf[NB][4];
  auto hld = [&](int t, u32x4 (&d)[4]) {
    const char* hp = hb + t * 2048;
    asm volatile("global_load_dwordx4 %0, %4, %5 sc1\n\tglobal_load_dwordx4 %1, %4, %6 sc1\n\tglobal_load_dwordx4 %2, %4, %5 offset:1024 sc1\n\tglobal_load_dwordx4 %3, %4, %6 offset:1024 sc1"
                 : "=&v"(d[0]), "=&v"(d[1]), "=&v"(d[2]), "=&v"(d[3]) : "v"(lo), "s"(hp), "s"(hp + 112 * 1024) : "memory");
  };
#pragma unroll
  for (int t = 0; t < PH; ++t) hld(t, hf[t]);
  LAS char* wbA[2]; LAS char* wbB[2];
#pragma unroll
  for (int c = 0; c < 2; ++c) { const u32 lo_ = (4 * h + q) * 128 + ((c ^ ((q >> 1) & 1)) << 6) + blk * 32 + p * 8; wbA[c] = sm + wv * 8192 + lo_; wbB[c] = sm + 32768 + wv * 16384 - 8192 + lo_; }
  f32x16 ac2[2][2] = {};
#pragma unroll
  for (int t = 0; t < 14; ++t) {
    int allow = 0;
    if (t < PH) { allow = 4 * (PH - 1 - t); for (int i = 0; i < t; ++i) allow += ((i + PH < 14) ? 4 : 0) + ((i >= 1 && i + 5 < 14) ? 4 : 0); }
    else { const int i0 = t - PH; allow = (i0 >= 1 && i0 + 5 < 14) ? 4 : 0; for (int i = i0 + 1; i < t; ++i) allow += ((i + PH < 14) ? 4 : 0) + ((i >= 1 && i + 5 < 14) ? 4 : 0); }
    u32x4 (&d)[4] = hf[t % NB];
    asm volatile("s_waitcnt vmcnt(%4)" : "+v"(d[0]), "+v"(d[1]), "+v"(d[2]), "+v"(d[3]) : "i"(allow) : "memory");
    s16x4 wf[2][2][2];
#pragma unroll
    for (int s2 = 0; s2 < 2; ++s2)
#pragma unroll
      for (int c = 0; c < 2; ++c) {
        wf[s2][c][0] = __builtin_amdgcn_ds_read_tr16_b64_v4i16((LAS s16x4*)(((t % 6) < 2 ? wbA[c] : wbB[c]) + (t % 6) * 4096 + s2 * 2048));
        wf[s2][c][1] = __builtin_amdgcn_ds_read_tr16_b64_v4i16((LAS s16x4*)(((t % 6) < 2 ? wbA[c] : wbB[c]) + (t % 6) * 4096 + s2 * 2048 + 1024));
      }
    __builtin_amdgcn_sched_barrier(0);
    if (t + PH < 14) hld(t + PH, hf[(t + PH) % NB]);
    __builtin_amdgcn_sched_barrier(0);
#pragma unroll
    for (int s2 = 0; s2 < 2; ++s2) {
#pragma unroll
      for (int c = 0; c < 2; ++c)
#pragma unroll
        for (int rt = 0; rt < 2; ++rt)
          ac2[c][rt] = __builtin_amdgcn_mfma_f32_32x32x16_bf16(__builtin_bit_cast(bf16x8, __builtin_shufflevector(wf[s2][c][0], wf[s2][c][1], 0, 1, 2, 3, 4, 5, 6, 7)),
                                                               __builtin_bit_cast(bf16x8, d[2 * s2 + rt]), ac2[c][rt], 0, 0, 0);
      __builtin_amdgcn_sched_barrier(0);
      if (s2 == 0 && t >= 1 && t + 5 < 14) wdma(t + 5);
      __builtin_amdgcn_sched_barrier(0);
    }
  }
  STAMP(17);
  lds_set(sm, K1_FL + 0 + wv);
  lds_wait4(sm, K1_FL + 0);
  LAS char* rg = sm + K2_RED + wv * 17408;
#pragma unroll
  for (int c = 0; c < 2; ++c)
#pragma unroll
    for (int rt = 0; rt < 2; ++rt)
#pragma unroll
      for (int g = 0; g < 4; ++g)
        *(LAS f32x4*)(rg + (32 * rt + r32) * 272 + (32 * c + 8 * g + 4 * h) * 4) = (f32x4){ac2[c][rt][4 * g], ac2[c][rt][4 * g + 1], ac2[c][rt][4 * g + 2], ac2[c][rt][4 * g + 3]};
  STAMP(18); RSTAMP(29);
}

DI float swap32_sum(float x) { auto r = __builtin_amdgcn_permlane32_swap(__float_as_uint(x), __float_as_uint(x), false, false); return __uint_as_float(r[0]) + __uint_as_float(r[1]); }
DI float swap16_sum(float x) { auto r = __builtin_amdgcn_permlane16_swap(__float_as_uint(x), __float_as_uint(x), false, false); return __uint_as_float(r[0]) + __uint_as_float(r[1]); }
struct SsmIn { u32 ldt, dsk, la, ai, cr, ci; u32x2 c4r, c4i; u32x4 lut0; u32x2 lut1; };
DI void ssm_load(SsmIn& in, int kw, int lane, int d0, const u16* logA, const u16* Aim, const u16* Cr, const u16* Ci, const u16* logdt, const u16* Dskip) {
  const int ch = kw >> 2, wv = kw & 3, d = d0 + ch;
  const int n = d * NS + wv * 64 + lane;
  in.ldt = logdt[d]; in.la = logA[n]; in.ai = Aim[n]; in.cr = Cr[n]; in.ci = Ci[n];
  in.c4r = *(const u32x2*)(Cr + d * NS + 4 * lane); in.c4i = *(const u32x2*)(Ci + d * NS + 4 * lane);
  in.dsk = Dskip[d];
  const u16* lp = &g_lut.v[ch][wv * 64 + lane][0];
  in.lut0 = *(const u32x4*)lp; in.lut1 = *(const u32x2*)(lp + 8);
}
#define DSR64(dst, base, off) asm volatile("ds_read_b64 %0, %1 offset:%2" : "=v"(dst) : "v"(base), "i"(off) : "memory")
#define DSR128(dst, base, off) asm volatile("ds_read_b128 %0, %1 offset:%2" : "=v"(dst) : "v"(base), "i"(off) : "memory")

DI void h_role(LAS char* sm, int kw, int lane, int bid, int pm, int pn, u32& tag, const u16* x, const u16* w1,
               const u16* logA, const u16* Aim, const u16* Cr, const u16* Ci, const u16* logdt, const u16* Dskip,
               char* UT, char* RST, u16* ST, u32* flags DBG_ARG) {
  STAMP(0);
  const int d0 = 2 * bid;
  const int wv = kw & 3, ch = kw >> 2, t = wv * 64 + lane;
  const int r32 = lane & 31, h = lane >> 5;
  const u32 smb = (u32)(size_t)sm;
  u32x4 xrow = *(const u32x4*)(x + (size_t)((pm * 16 + pn) * 8 + min(kw, 5)) * D + 8 * min(lane, 55));
  u32 raw = epoch_load(flags);
  SsmIn in;
  ssm_load(in, kw, lane, d0, logA, Aim, Cr, Ci, logdt, Dskip);
  CBAR();
  if (kw >= 6) {
    const u32 woff = (u32)((32 * (kw - 6) + (lane >> 4)) * (FF * 2) + pn * 256 + (((lane & 15) ^ (((lane >> 4) & 3) << 2)) << 4));
#pragma unroll
    for (int st = 0; st < 7; ++st) {
#pragma unroll
      for (int i = 0; i < 8; ++i) glds16((const char*)w1 + (st * 64 + 4 * i) * (FF * 2), woff, smb + G1_W + st * 16384 + (kw - 6) * 8192 + i * 1024);
      if (st >= 1) { WAIT_VM(8); *(volatile LAS u16*)(sm + K1_FL + 128 + 4 * (st - 1) + 2 * (kw - 6)) = 0x0101; }
    }
    WAIT_VM(0); *(volatile LAS u16*)(sm + K1_FL + 128 + 4 * 6 + 2 * (kw - 6)) = 0x0101;
  } else {
    __builtin_amdgcn_s_setprio(3);
    ln_group_row(sm, xrow, lane, kw, pm * 16 + pn, UT, RST);
    __builtin_amdgcn_s_setprio(0);
  }
  STAMP(4);
#define SEG() __builtin_amdgcn_sched_barrier(0)
  const float dt = rbf(exp_bf(__uint_as_float(in.ldt << 16)));
  float SG, F, invK;
  kgen_scales(dt, in.c4r, in.c4i, SG, F, invK);
  u32 o[53];
  const float zr = -rbf(exp_bf(__uint_as_float(in.la << 16))) * dt, zi = __uint_as_float(in.ai << 16) * dt;
  cf E8 = cexp1(zr, zi, 8.f), e1 = cexp1(zr, -zi, 1.f), e256 = cexp1(zr, -zi, 256.f);
  cm m8 = mk(E8);
  cf E16 = cmul1(E8, m8); cm m16 = mk(E16);
  cf E24 = cmul1(E16, m8), E32 = cmul1(E16, m16); cm m32 = mk(E32);
  cm m64 = mk(cmul1(E32, m32));
  const u32 p8 = pkc(E8), r8 = pkr(E8), p16 = pkc(E16), r16 = pkr(E16), p24 = pkc(E24), r24 = pkr(E24);
  cf A0 = {16384.f, 0.f}, A1 = {E32.r * 16384.f, E32.i * 16384.f};
  auto hrows = [&](int i) {
    const u32 a0 = pkc(A0), a1 = pkc(A1);
    o[8 * i + 0] = a0; o[8 * i + 1] = hmul(a0, p8, r8); o[8 * i + 2] = hmul(a0, p16, r16); o[8 * i + 3] = hmul(a0, p24, r24);
    o[8 * i + 4] = a1; o[8 * i + 5] = hmul(a1, p8, r8); o[8 * i + 6] = hmul(a1, p16, r16); o[8 * i + 7] = hmul(a1, p24, r24);
    if (i < 3) { A0 = cmul1(A0, m64); A1 = cmul1(A1, m64); }
  };
  hrows(0); hrows(1);
  SEG();
  WAIT_VM(0); TAG_READY(raw, tag);
  if (kw < 6) { ln_publish(sm, lane, flags + FL_U + (pm * 16 + pn) * FSP, tag); }
  STAMP(1);
  SEG();
  hrows(2); hrows(3);
  cm m1 = mk(e1), m256 = mk(e256);
  cf e2 = cmul1(e1, m1); cm m2 = mk(e2);
  cf e3 = cmul1(e2, m1), e4 = cmul1(e2, m2);
  const u32 q1 = pkc(e1), s1 = pkr(e1), q2 = pkc(e2), s2 = pkr(e2), q3 = pkc(e3), s3 = pkr(e3);
  cf g0 = {__uint_as_float(in.cr << 16) * dt * SG, -(__uint_as_float(in.ci << 16) * dt) * SG};
  cf g4 = cmul1(g0, mk(e4));
  u32 b0 = pkc(g0), b4 = pkc(g4);
  o[32 + 0] = b0; o[32 + 1] = hmul(b0, q1, s1); o[32 + 2] = hmul(b0, q2, s2); o[32 + 3] = hmul(b0, q3, s3);
  o[32 + 4] = b4; o[32 + 5] = hmul(b4, q1, s1); o[32 + 6] = hmul(b4, q2, s2); o[32 + 7] = hmul(b4, q3, s3);
  g0 = cmul1(g0, m256); g4 = cmul1(g4, m256);
  b0 = pkc(g0); b4 = pkc(g4);
  o[32 + 8] = hmul(b0, q2, s2); o[32 + 9] = b4; o[32 + 10] = hmul(b4, q2, s2); o[32 + 11] = b0;
  g0 = cmul1(g0, m256); g4 = cmul1(g4, m256);
  o[32 + 12] = pkc(g0); o[32 + 19] = pkc(g4);
  g0 = cmul1(g0, m256); g4 = cmul1(g4, m256);
  o[32 + 13] = pkc(g0); o[32 + 20] = pkc(g4);
#pragma unroll
  for (int k = 4; k <= 8; ++k) { g0 = cmul1(g0, m256); o[42 + k] = pkc(g0); }
  f32x16 tacc = {};
  LAS char* kg = sm + G1_W + (kw < 6 ? kw : kw - 6) * 16384;
  auto round = [&]() {
    LAS u32* hw = (LAS u32*)(kg + lane * 4);
#pragma unroll
    for (int i = 0; i < 53; ++i) hw[i * (KG_ROW / 4)] = o[i];
    LAS char* pa = kg + (32 + min(r32, 20)) * KG_ROW + h * 16;
    LAS char* pb = kg + r32 * KG_ROW + h * 16;
    h16x8 fa[8], fb[8];
#pragma unroll
    for (int ks = 0; ks < 8; ++ks) { fa[ks] = *(LAS h16x8*)(pa + ks * 32); fb[ks] = *(LAS h16x8*)(pb + ks * 32); }
#pragma unroll
    for (int ks = 0; ks < 8; ++ks) tacc = __builtin_amdgcn_mfma_f32_32x32x16_f16(fa[ks], fb[ks], tacc, 0, 0, 0);
    WAIT_LGKM0();
  };
  asm volatile("" : "+v"(o[0]), "+v"(o[1]), "+v"(o[2]), "+v"(o[3]), "+v"(o[4]), "+v"(o[5]), "+v"(o[6]), "+v"(o[7]), "+v"(o[8]), "+v"(o[9]), "+v"(o[10]), "+v"(o[11]), "+v"(o[12]), "+v"(o[13]), "+v"(o[14]), "+v"(o[15]), "+v"(o[16]), "+v"(o[17]), "+v"(o[18]), "+v"(o[19]), "+v"(o[20]), "+v"(o[21]), "+v"(o[22]), "+v"(o[23]), "+v"(o[24]), "+v"(o[25]), "+v"(o[26]));
  asm volatile("" : "+v"(o[27]), "+v"(o[28]), "+v"(o[29]), "+v"(o[30]), "+v"(o[31]), "+v"(o[32]), "+v"(o[33]), "+v"(o[34]), "+v"(o[35]), "+v"(o[36]), "+v"(o[37]), "+v"(o[38]), "+v"(o[39]), "+v"(o[40]), "+v"(o[41]), "+v"(o[42]), "+v"(o[43]), "+v"(o[44]), "+v"(o[45]), "+v"(o[46]), "+v"(o[47]), "+v"(o[48]), "+v"(o[49]), "+v"(o[50]), "+v"(o[51]), "+v"(o[52]));
  STAMP(2);
  lds_wait4(sm, K1_FL + (kw < 6 ? 192 + 4 * kw : 216 + 4 * (kw - 6)));
  round();
  if (kw < 2) *(volatile LAS u32*)(sm + K1_FL + 216 + 4 * kw) = 0x01010101u;
  STAMP(5);
  if (kw == 0) {
    __builtin_amdgcn_s_setprio(3);
    poll_flags(flags + FL_U + (pm * 16 + (lane & 15)) * FSP, tag);
    const __amdgpu_buffer_rsrc_t srs = __builtin_amdgcn_make_buffer_rsrc(RST, 0, L * 8, 0x00020000);
    u32x4 sv = __builtin_amdgcn_raw_buffer_load_b128(srs, (u32)((pm * 128 + 2 * lane) * 8), 0, 16);
    *(LAS u32x4*)(sm + G1_RS + lane * 16) = sv;
    WAIT_LGKM0();
    *(volatile LAS u32*)(sm + K1_FL + 176) = 0x01010101u;
    __builtin_amdgcn_s_setprio(0);
  }
  if (kw >= 1 && kw <= 4) { __builtin_amdgcn_s_setprio(3); poll_flags(flags + FL_U + ((kw - 1) * 64 + lane) * FSP, tag); __builtin_amdgcn_s_setprio(0); }
  lds_wait4(sm, K1_FL + 180);
  STAMP(3);
#pragma unroll
  for (int i = 0; i < 12; ++i) {
    const int a = (i & 3) + 8 * (i >> 2) + 4 * h;
    if (a < 22) *(LAS float*)(sm + K2_PT + kw * 2816 + (a * 32 + r32) * 4) = tacc[i];
  }
  STAMP(8);
  lds_bar8(sm, K1_FL + 64, kw);
  STAMP(9);
  u32x4 uv;
  {
    __builtin_amdgcn_s_setprio(3);
    const __amdgpu_buffer_rsrc_t urs = __builtin_amdgcn_make_buffer_rsrc(UT, 0, 256 * UG, 0x00020000);
    uv = __builtin_amdgcn_raw_buffer_load_b128(urs, (u32)((4 * lane + wv) * UG + (d0 + ch) * 16), 0, 16);
    __builtin_amdgcn_s_setprio(0);
  }
  {
    LAS char* pb0 = sm + K2_PT + ch * 4 * 2816 + t * 16;
    const u32 st = 2816;
    f32x4 v = {0.f, 0.f, 0.f, 0.f};
    if (t < 176) v = ((*(LAS f32x4*)(pb0) + *(LAS f32x4*)(pb0 + st)) + (*(LAS f32x4*)(pb0 + 2 * st) + *(LAS f32x4*)(pb0 + 3 * st))) * F;
    if (t < 184) *(LAS f32x4*)(sm + K2_T + ch * 2944 + t * 16) = v;
  }
  STAMP(10);
  lds_bar8(sm, K1_FL + 72, kw);
  STAMP(11);
  {
    float v[11];
#pragma unroll
    for (int e = 0; e < 11; ++e) {
      u32 w = e < 8 ? in.lut0[e >> 1] : in.lut1[(e - 8) >> 1];
      u32 off = (e & 1) ? (w >> 16) : (w & 0xffffu);
      v[e] = *(LAS float*)(sm + K2_T + off);
    }
    u32 ev[5], od[5];
#pragma unroll
    for (int i = 0; i < 5; ++i) { ev[i] = pk_f16((f32x2){v[2 * i], v[2 * i + 1]}); od[i] = pk_f16((f32x2){v[2 * i + 1], v[2 * i + 2]}); }
    LAS char* kt = sm + CV_K + ch * KT_BYTES + t * 16;
    *(LAS u32x4*)(kt + 3 * KT_COPY) = (u32x4){ev[0], ev[1], ev[2], ev[3]};
    *(LAS u32x4*)(kt + 2 * KT_COPY) = (u32x4){od[0], od[1], od[2], od[3]};
    *(LAS u32x4*)(kt + 1 * KT_COPY) = (u32x4){ev[1], ev[2], ev[3], ev[4]};
    *(LAS u32x4*)(kt + 0 * KT_COPY) = (u32x4){od[1], od[2], od[3], od[4]};
    if (lane < 32) *(LAS u32x4*)(sm + CV_U + ch * 6144 + wv * 1536 + lane * 16) = (u32x4){0u, 0u, 0u, 0u};
    if (t < 16) *(LAS u32x4*)(sm + CV_K + ch * KT_BYTES + (t >> 2) * KT_COPY + 4096 + (t & 3) * 16) = (u32x4){0u, 0u, 0u, 0u};
  }
  STAMP(12);
  *(LAS u32x4*)(sm + CV_U + ch * 6144 + wv * 1536 + 512 + lane * 16) = uv;
  lds_bar8(sm, K1_FL + 80, kw);
  STAMP(13);
  f32x16 accA = {}, accB = {};
  {
    const u32 ka = smb + CV_K + ch * KT_BYTES + (r32 & 3) * KT_COPY + (2044 + 8 * h - 4 * (r32 >> 2)) * 2 - 7 * 64;
    const u32 ub = smb + CV_U + ch * 6144 + h * 1536 + (32 + r32) * 16 - 7 * 16;
    u32 kb[3], uu[3];
#pragma unroll
    for (int i = 0; i < 3; ++i) { const int sg = 3 * wv + i, g = sg >= 4, dl0 = 8 * (g ? sg - 4 : sg); kb[i] = ka - dl0 * 64; uu[i] = ub + (32 * g - dl0) * 16; }
    u32x2 a0[2][4], a1[2][4]; u32x4 bb[2][4];
#define CV_RD(n, buf, m) do { DSR64(a0[buf][m], kb[(n) >> 2], (7 - 2 * ((n) & 3) - ((m) >> 1)) * 64 + ((m) & 1) * 32); \
                              DSR64(a1[buf][m], kb[(n) >> 2], (7 - 2 * ((n) & 3) - ((m) >> 1)) * 64 + ((m) & 1) * 32 + 8); \
                              DSR128(bb[buf][m], uu[(n) >> 2], (7 - 2 * ((n) & 3) - ((m) >> 1)) * 16 + ((m) & 1) * 3072); } while (0)
#define CV_WAIT(buf, cnt) asm volatile("s_waitcnt lgkmcnt(" #cnt ")" : "+v"(a0[buf][0]), "+v"(a0[buf][1]), "+v"(a0[buf][2]), "+v"(a0[buf][3]), "+v"(a1[buf][0]), "+v"(a1[buf][1]), "+v"(a1[buf][2]), "+v"(a1[buf][3]), \
                                       "+v"(bb[buf][0]), "+v"(bb[buf][1]), "+v"(bb[buf][2]), "+v"(bb[buf][3]) :: "memory")
#define CV_MM(acc, buf, m) acc = __builtin_amdgcn_mfma_f32_32x32x16_f16(__builtin_bit_cast(h16x8, (u32x4){a0[buf][m].x, a0[buf][m].y, a1[buf][m].x, a1[buf][m].y}), __builtin_bit_cast(h16x8, bb[buf][m]), acc, 0, 0, 0)
#define CV_BATCH(n, acc) do { CV_WAIT((n) & 1, 0); \
      CV_MM(acc, (n) & 1, 0); __builtin_amdgcn_sched_barrier(0); if ((n) < 11) { CV_RD((n) + 1, ((n) + 1) & 1, 0); CV_RD((n) + 1, ((n) + 1) & 1, 1); CV_RD((n) + 1, ((n) + 1) & 1, 2); CV_RD((n) + 1, ((n) + 1) & 1, 3); } __builtin_amdgcn_sched_barrier(0); \
      CV_MM(acc, (n) & 1, 1); CV_MM(acc, (n) & 1, 2); CV_MM(acc, (n) & 1, 3); __builtin_amdgcn_sched_barrier(0); } while (0)
    CV_RD(0, 0, 0); CV_RD(0, 0, 1); CV_RD(0, 0, 2); CV_RD(0, 0, 3);
    CV_BATCH(0, accA); CV_BATCH(1, accA); CV_BATCH(2, accA); CV_BATCH(3, accA);
    CV_BATCH(4, accB); CV_BATCH(5, accB); CV_BATCH(6, accB); CV_BATCH(7, accB);
    CV_BATCH(8, accB); CV_BATCH(9, accB); CV_BATCH(10, accB); CV_BATCH(11, accB);
  }
  STAMP(14);
  lds_bar8(sm, K1_FL + 96, kw);
  {
    const int slot = wv == 0 ? 0 : wv + 1;
    LAS char* pp = sm + CV_P + ch * 20480 + slot * 4096 + r32 * 128;
    if (wv == 1) {
#pragma unroll
      for (int g = 0; g < 4; ++g) *(LAS f32x4*)(pp - 4096 + (((2 * g + h) ^ (r32 & 7)) << 4)) = (f32x4){accA[4 * g], accA[4 * g + 1], accA[4 * g + 2], accA[4 * g + 3]};
    } else {
      accB += accA;
    }
#pragma unroll
    for (int g = 0; g < 4; ++g) *(LAS f32x4*)(pp + (((2 * g + h) ^ (r32 & 7)) << 4)) = (f32x4){accB[4 * g], accB[4 * g + 1], accB[4 * g + 2], accB[4 * g + 3]};
  }
  lds_bar8(sm, K1_FL + 104, kw);
  STAMP(15);
  {
    const int a = t >> 2, bq = t & 3, g = a >> 5, ar = a & 31;
    LAS char* pp = sm + CV_P + ch * 20480 + (g ? 2 * 4096 : 0) + ar * 128;
    const u32 o0 = ((2 * bq) ^ (ar & 7)) << 4, o1 = ((2 * bq + 1) ^ (ar & 7)) << 4;
    f32x4 y0 = *(LAS f32x4*)(pp + o0) + *(LAS f32x4*)(pp + 4096 + o0);
    f32x4 y1 = *(LAS f32x4*)(pp + o1) + *(LAS f32x4*)(pp + 4096 + o1);
    if (g) { y0 += *(LAS f32x4*)(pp + 8192 + o0); y1 += *(LAS f32x4*)(pp + 8192 + o1); }
    h16x8 u = *(LAS h16x8*)(sm + CV_U + ch * 6144 + bq * 1536 + (32 + a) * 16);
    const float ds = __uint_as_float(in.dsk << 16);
    float r[8];
#pragma unroll
    for (int i = 0; i < 8; ++i) { float y = (i < 4 ? y0[i & 3] : y1[i & 3]) * invK; r[i] = rbf(y) + rbf((float)u[i] * ds); }
    u32x4 ov = {pk_bf16(r[0], r[1]), pk_bf16(r[2], r[3]), pk_bf16(r[4], r[5]), pk_bf16(r[6], r[7])};
    const __amdgpu_buffer_rsrc_t rs = __builtin_amdgcn_make_buffer_rsrc(ST, 0, D * LP * 2, 0x00020000);
    __builtin_amdgcn_s_setprio(3);
    __builtin_amdgcn_raw_buffer_store_b128(ov, rs, (u32)(((d0 + ch) * LP + 8 * t) * 2), 0, 16);
  }
  WAIT_VM(0);
  {
    u32 old = 0;
    if (lane == 0) old = __hip_atomic_fetch_add((LAS u32*)(sm + K1_FL + 88), 1u, __ATOMIC_RELAXED, __HIP_MEMORY_SCOPE_WORKGROUP);
    if (lane == 0 && old == 7) __hip_atomic_store((gu32*)(flags + FL_S + bid * FSP), tag, RLX_AGENT);
  }
  STAMP(16); RSTAMP(29);
}

__global__ __launch_bounds__(768, 3) void p_kernel(const u16* x, const u16* logA, const u16* Aim, const u16* Cr, const u16* Ci, const u16* logdt,
                                                   const u16* Dskip, const u16* w1, const u16* w2, char* ws, u16* out) {
  extern __shared__ __attribute__((aligned(16))) char smem[];
  LAS char* sm = (LAS char*)smem;
  const int tid = threadIdx.x, lane = tid & 63;
  const int wave = __builtin_amdgcn_readfirstlane(tid >> 6);
  const int bid = blockIdx.x;
  RSTAMP(30);
  const int xcd = bid & 7, idx = bid >> 3;
  const int pm = xcd * 2 + (idx & 1), pn = idx >> 1;
  const int pm2 = 2 * pm + (pn >= 7), pc = pn >= 7 ? pn - 7 : pn;
  u32* flags = (u32*)(ws + WS_FLAG);
  u32 tag = 0;
  if (tid < 64) *(LAS u32*)(sm + K1_FL + tid * 4) = 0u;
  if (bid >= 224) {
    WAIT_LGKM0(); CBAR();
    if (wave < 8) {
      const int e = bid - 224, g = (e >> 1) * 16 + 14 + (e & 1);
      u32x4 xrow = *(const u32x4*)(x + (size_t)(g * 8 + wave) * D + 8 * min(lane, 55));
      tag = epoch_load(flags) + 1u;
      ln_group_row(sm, xrow, lane, wave, g, ws + WS_UT, ws + WS_RINV);
      WAIT_VM(0);
      ln_publish(sm, lane, flags + FL_U + g * FSP, tag);
    }
    return;
  }
  if (wave < 4) {
    m_role(sm, wave, lane, pm, pn, tag, x, w1, ws + WS_UT, ws + WS_RINV, ws + WS_H, w2, flags DBG_PASS);
  } else {
    h_role(sm, wave - 4, lane, xcd * 28 + idx, pm, pn, tag, x, w1, logA, Aim, Cr, Ci, logdt, Dskip, ws + WS_UT, ws + WS_RINV, (u16*)(ws + WS_ST), flags DBG_PASS);
    if (wave == 4) poll_flags(flags + FL_S + (pc * 32 + (lane & 31)) * FSP, tag);
  }
  STAMP(20);
  WAIT_LGKM0(); CBAR();
  STAMP(21);
  if (wave >= 4) {
    const int e = tid - 256;
    const __amdgpu_buffer_rsrc_t rs = __builtin_amdgcn_make_buffer_rsrc(ws + WS_ST, 0, D * LP * 2, 0x00020000);
    u32x4 sv = __builtin_amdgcn_raw_buffer_load_b128(rs, (u32)(((pc * 64 + (e >> 3)) * LP + pm2 * 64 + 8 * (e & 7)) * 2), 0, 16);
    const int row = e >> 3, cq = e & 7;
    u32x4 xv = *(const u32x4*)(x + (size_t)(pm2 * 64 + row) * D + pc * 64 + 8 * cq);
    float rinv = *(LAS float*)(sm + G1_RS + ((pn >= 7 ? 64 : 0) + row) * 8 + 4);
    f32x4 z0 = {0.f, 0.f, 0.f, 0.f}, z1 = {0.f, 0.f, 0.f, 0.f};
#pragma unroll
    for (int w = 0; w < 4; ++w) {
      z0 += *(LAS f32x4*)(sm + K2_RED + w * 17408 + row * 272 + cq * 32);
      z1 += *(LAS f32x4*)(sm + K2_RED + w * 17408 + row * 272 + cq * 32 + 16);
    }
    *(LAS u32x4*)(sm + K2_S + (e >> 3) * 144 + (e & 7) * 16) = sv;
    lds_bar8(sm, K1_FL + 112, wave - 4);
    float r[8];
#pragma unroll
    for (int i = 0; i < 8; ++i) {
      float sval = bf2f(*(LAS u16*)(sm + K2_S + (8 * cq + i) * 144 + row * 2));
      float o = rbf((i < 4 ? z0[i & 3] : z1[i & 3]) * rinv);
      float g = rbf(o * __builtin_amdgcn_rcpf(1.f + __builtin_amdgcn_exp2f(-1.4426950408889634f * o)));
      float xx = (i & 1) ? __uint_as_float(xv[i >> 1] & 0xffff0000u) : __uint_as_float(xv[i >> 1] << 16);
      r[i] = xx + rbf(sval * g);
    }
    *(u32x4*)(out + (size_t)(pm2 * 64 + row) * D + pc * 64 + 8 * cq) = (u32x4){pk_bf16(r[0], r[1]), pk_bf16(r[2], r[3]), pk_bf16(r[4], r[5]), pk_bf16(r[6], r[7])};
  } else if (bid == 0 && wave == 0) {
#pragma unroll 1
    for (int i = 0; i < 4; ++i) poll_flags(flags + FL_S + min(i * 64 + lane, 223) * FSP, tag);
    if (lane == 0) __hip_atomic_store((gu32*)flags, tag, RLX_AGENT);
  }
  STAMP(22);
  STAMP_DUMP();
}
}

#define LDSZ(x) (x)
extern "C" void kernel_launch(void* const* d_in, const int* in_sizes, int n_in,
                              void* d_out, int out_size, void* d_ws, size_t ws_size,
                              hipStream_t stream) {
  (void)hipFuncSetAttribute((const void*)p_kernel, hipFuncAttributeMaxDynamicSharedMemorySize, LDSZ(P_LDS));
  p_kernel<<<256, 768, LDSZ(P_LDS), stream>>>((const u16*)d_in[0], (const u16*)d_in[1], (const u16*)d_in[2], (const u16*)d_in[3], (const u16*)d_in[4], (const u16*)d_in[5],
                                              (const u16*)d_in[6], (const u16*)d_in[8], (const u16*)d_in[9], (char*)d_ws, (u16*)d_out);
}
```

```cpp
#include <hip/hip_runtime.h>
#include <hip/hip_bf16.h>
#include <stdint.h>

namespace {
constexpr int L = 2048, D = 448, NS = 256, FF = 1792;
typedef unsigned short u16;
typedef unsigned int u32;
typedef __attribute__((ext_vector_type(2))) float f32x2;
typedef __attribute__((ext_vector_type(4))) float f32x4;
typedef __attribute__((ext_vector_type(16))) float f32x16;
typedef __attribute__((ext_vector_type(2))) _Float16 h16x2;
typedef __attribute__((ext_vector_type(8))) _Float16 h16x8;
typedef __attribute__((ext_vector_type(2))) __bf16 bf16x2;
typedef __attribute__((ext_vector_type(8))) __bf16 bf16x8;
typedef __attribute__((ext_vector_type(4))) short s16x4;
typedef __attribute__((ext_vector_type(8))) short s16x8;
typedef __attribute__((ext_vector_type(2))) u32 u32x2;
typedef __attribute__((ext_vector_type(4))) u32 u32x4;
#define LAS __attribute__((address_space(3)))
#define DI __device__ __forceinline__

constexpr size_t WS_UT = 0;
constexpr size_t WS_KT = 2u << 20;
constexpr size_t WS_KS = 10u << 20;
constexpr size_t WS_H = 4u << 20;
constexpr size_t WS_ST = 2u << 20;
constexpr size_t WS_FLAG = (2u << 20) - (128u << 10);
constexpr size_t WS_RINV = (2u << 20) - (160u << 10);
constexpr size_t WS_W2F = 26u << 20;
constexpr int LP = L + 128;
constexpr int FSP = 16;
constexpr int KT_COPY = 4160;
constexpr int KT_BYTES = 4 * KT_COPY;

DI float bf2f(u16 v) { return __uint_as_float(((u32)v) << 16); }
DI float rbf(float f) { return (float)(__bf16)f; }
DI u32 pk_bf16(float a, float b) { f32x2 v = {a, b}; return __builtin_bit_cast(u32, __builtin_convertvector(v, bf16x2)); }
DI u32 pk_f16(f32x2 v) { return __builtin_bit_cast(u32, __builtin_convertvector(v, h16x2)); }

DI f32x2 cmul(f32x2 a, f32x2 b, f32x2 bs) {
  f32x2 t = a.xx * b;
  return __builtin_elementwise_fma(a.yy, bs, t);
}
DI f32x2 rot(f32x2 b) { f32x2 r = {-b.y, b.x}; return r; }

template <int CTRL> DI int dpp_i(int x) { return __builtin_amdgcn_update_dpp(0, x, CTRL, 0xf, 0xf, false); }
DI float wave_sum(float x) {
  x += __builtin_bit_cast(float, dpp_i<0xB1>(__builtin_bit_cast(int, x)));
  x += __builtin_bit_cast(float, dpp_i<0x4E>(__builtin_bit_cast(int, x)));
  x += __builtin_bit_cast(float, dpp_i<0x141>(__builtin_bit_cast(int, x)));
  x += __builtin_bit_cast(float, dpp_i<0x140>(__builtin_bit_cast(int, x)));
  int xi = __builtin_bit_cast(int, x);
  float a = __builtin_bit_cast(float, __builtin_amdgcn_readlane(xi, 0)), b = __builtin_bit_cast(float, __builtin_amdgcn_readlane(xi, 16));
  float c = __builtin_bit_cast(float, __builtin_amdgcn_readlane(xi, 32)), d = __builtin_bit_cast(float, __builtin_amdgcn_readlane(xi, 48));
  return (a + b) + (c + d);
}
DI int wave_max_i(int x) {
  x = max(x, dpp_i<0xB1>(x)); x = max(x, dpp_i<0x4E>(x)); x = max(x, dpp_i<0x141>(x)); x = max(x, dpp_i<0x140>(x));
  return max(max(__builtin_amdgcn_readlane(x, 0), __builtin_amdgcn_readlane(x, 16)), max(__builtin_amdgcn_readlane(x, 32), __builtin_amdgcn_readlane(x, 48)));
}

DI void glds16(const void* sbase, u32 voff, u32 ldst) {
  u32 keep;
  asm volatile("s_mov_b32 %0, m0\n\ts_mov_b32 m0, %3\n\ts_nop 0\n\tglobal_load_lds_dwordx4 %1, %2\n\ts_mov_b32 m0, %0"
               : "=&s"(keep) : "v"(voff), "s"(sbase), "s"(ldst) : "memory");
}
#define WAIT_VM(n) asm volatile("s_waitcnt vmcnt(" #n ")" ::: "memory")
#define WAIT_LGKM0() asm volatile("s_waitcnt lgkmcnt(0)" ::: "memory")

DI void lds_wait4(LAS char* sm, u32 off) {
  u32 addr = (u32)(size_t)sm + off, tv, td, ts;
  asm volatile("v_mov_b32 %0, %3\n"
               "WL%=:\n\ts_sleep 1\n\tds_read_b32 %1, %0\n\ts_waitcnt lgkmcnt(0)\n\tv_readfirstlane_b32 %2, %1\n\ts_cmp_lg_u32 %2, 0x01010101\n\ts_cbranch_scc1 WL%=\n"
               : "=&v"(tv), "=&v"(td), "=&s"(ts) : "s"(addr) : "memory", "scc");
}
DI u32 flag_pre(LAS char* sm, u32 off) { return *(volatile LAS u32*)(sm + off); }
DI void flag_chk(u32 v, LAS char* sm, u32 off) {
  u32 addr = (u32)(size_t)sm + off, tv, ts;
  asm volatile("v_readfirstlane_b32 %2, %0\n\ts_cmp_eq_u32 %2, 0x01010101\n\ts_cbranch_scc1 FD%=\n\tv_mov_b32 %1, %3\n"
               "FL%=:\n\tds_read_b32 %0, %1\n\ts_waitcnt lgkmcnt(0)\n\tv_readfirstlane_b32 %2, %0\n\ts_cmp_lg_u32 %2, 0x01010101\n\ts_cbranch_scc1 FL%=\nFD%=:\n"
               : "+v"(v), "=&v"(tv), "=&s"(ts) : "s"(addr) : "memory", "scc");
}
DI void lds_set(LAS char* sm, u32 off) {
  *(volatile LAS unsigned char*)(sm + off) = 1;
}
DI void lds_bar4(LAS char* sm, u32 off, u32 target, int lane) {
  WAIT_LGKM0();
  if (lane == 0) __hip_atomic_fetch_add((LAS u32*)(sm + off), 1u, __ATOMIC_RELAXED, __HIP_MEMORY_SCOPE_WORKGROUP);
  for (;;) {
    u32 v = *(volatile LAS u32*)(sm + off);
    if ((u32)__builtin_amdgcn_readfirstlane(v) >= target) break;
  }
  asm volatile("" ::: "memory");
}

#define STAMP(i)
#define STAMP_DUMP()
#define RSTAMP(i)
#define DBG_ARG
#define DBG_PASS
#define DBG_PASS1
#define DBG_PASS2
constexpr u32 G1_W = 0;
constexpr u32 G1_X = 114688;
constexpr u32 KG_ROW = 272, KG_SZ = 53 * KG_ROW;
constexpr u32 LN_T = 147456;
constexpr u32 K2_S = 131072;
constexpr u32 G1_CS = 159904;
constexpr u32 G1_RS = G1_CS + 1024;
constexpr u32 K1_FL = G1_RS + 1024;
constexpr u32 P_LDS = K1_FL + 256;
constexpr u32 K2_RED = 0;
constexpr u32 K2_PT = 110592;
constexpr u32 CV_U = 98304;
constexpr u32 CV_K = 110592;
constexpr u32 CV_P = 110592;
constexpr u32 K2_T = 151552;
constexpr int UG = 7424;
#define CBAR() do { asm volatile("" ::: "memory"); __builtin_amdgcn_s_barrier(); asm volatile("" ::: "memory"); } while (0)
typedef __attribute__((address_space(1))) u32 gu32;
#define RLX_AGENT __ATOMIC_RELAXED, __HIP_MEMORY_SCOPE_AGENT
constexpr int FL_H = 16 * FSP, FL_U = 1056 * FSP, FL_S = 1328 * FSP;
#define PSLEEP 8
DI u32 epoch_load(const u32* flags) { return __hip_atomic_load((const gu32*)flags, RLX_AGENT); }
#define TAG_READY(raw, tag) do { asm volatile("" : "+v"(raw)); tag = raw + 1u; } while (0)
DI void poll_flags(const u32* p, u32 tag) {
  for (u32 spins = 0; spins < (1u << 22); ++spins) {
    u32 v = __hip_atomic_load((const gu32*)p, RLX_AGENT);
    if (__all(v == tag)) break;
    __builtin_amdgcn_s_sleep(PSLEEP);
  }
  asm volatile("" ::: "memory");
}

struct LutT { u16 v[2][256][12]; };
constexpr LutT make_lut() {
  LutT t{};
  for (int ch = 0; ch < 2; ++ch)
    for (int th = 0; th < 256; ++th)
      for (int e = 0; e < 12; ++e) {
        int j = 2047 - 8 * th - e;
        int off = 22 * 128;
        if (j >= 0) {
          int p = j;
          if (j >= 256) { int sh = j >= 1024 ? 3 : (j >= 512 ? 2 : 1); int q = j >> sh, r = j & ((1 << sh) - 1), hf = 1 << (sh - 1); if (r > hf || (r == hf && (q & 1))) ++q; p = q << sh; }
          int b = (p & 255) >> 3, lo = p & 7, k = p >> 8, a = 0;
          if (k == 0) a = lo; else if (lo == 0) a = 10 + k; else if (k == 1) a = 7 + (lo >> 1); else a = 17 + k;
          off = (a * 32 + b) * 4;
        }
        t.v[ch][th][e] = (u16)(off + ch * 2944);
      }
  return t;
}
__device__ const LutT g_lut = make_lut();

DI f32x2 cexp_hw(float zr, float zi, float p) {
  float ph = zi * p;
  float k = __builtin_rintf(ph * 0.15915494309189535f);
  float r = __builtin_fmaf(ph, 0.15915494309189535f, -k);
  r = __builtin_fmaf(ph, 6.4206382433e-09f, r);
  float e = __builtin_amdgcn_exp2f(zr * p * 1.4426950408889634f);
  return (f32x2){e * __builtin_amdgcn_cosf(r), e * __builtin_amdgcn_sinf(r)};
}
DI void lds_bar8(LAS char* sm, u32 off, int kw) {
  WAIT_LGKM0();
  *(volatile LAS unsigned char*)(sm + off + kw) = 1;
  u32 addr = (u32)(size_t)sm + off, tv, td, te, ts;
  asm volatile("v_mov_b32 %0, %4\n"
               "WB%=:\n\ts_sleep 1\n\tds_read_b32 %1, %0\n\tds_read_b32 %2, %0 offset:4\n\ts_waitcnt lgkmcnt(0)\n\tv_and_b32 %1, %1, %2\n\ts_nop 0\n\tv_readfirstlane_b32 %3, %1\n\ts_cmp_lg_u32 %3, 0x01010101\n\ts_cbranch_scc1 WB%=\n"
               : "=&v"(tv), "=&v"(td), "=&v"(te), "=&s"(ts) : "s"(addr) : "memory", "scc");
}
DI void lds_bar8s(LAS char* sm, u32 off, int kw) {
  WAIT_LGKM0();
  *(volatile LAS unsigned char*)(sm + off + kw) = 1;
  u32 addr = (u32)(size_t)sm + off, tv, td, te, ts;
  asm volatile("v_mov_b32 %0, %4\n"
               "WS%=:\n\ts_sleep 2\n\tds_read_b32 %1, %0\n\tds_read_b32 %2, %0 offset:4\n\ts_waitcnt lgkmcnt(0)\n\tv_and_b32 %1, %1, %2\n\ts_nop 0\n\tv_readfirstlane_b32 %3, %1\n\ts_cmp_lg_u32 %3, 0x01010101\n\ts_cbranch_scc1 WS%=\n"
               : "=&v"(tv), "=&v"(td), "=&v"(te), "=&s"(ts) : "s"(addr) : "memory", "scc");
}
struct cf { float r, i; };
DI float amul(float a, float b) { float o; asm("v_mul_f32 %0, %1, %2" : "=v"(o) : "v"(a), "v"(b)); return o; }
DI void afmac(float& d, float a, float b) { asm("v_fmac_f32 %0, %1, %2" : "+v"(d) : "v"(a), "v"(b)); }
struct cm { float r, i, ni; };
DI cm mk(cf e) { return cm{e.r, e.i, -e.i}; }
DI cf cmul1(cf a, cm b) { cf o; o.r = amul(a.r, b.r); o.i = amul(a.r, b.i); afmac(o.r, a.i, b.ni); afmac(o.i, a.i, b.r); return o; }
DI u32 pkc(cf a) { return pk_f16((f32x2){a.r, a.i}); }
DI cf cexp1(float zr, float zi, float p) { f32x2 e = cexp_hw(zr, zi, p); return cf{e.x, e.y}; }

DI void kgen_scales(float dt, u32x2 c4r, u32x2 c4i, float& SG, float& F, float& invKS) {
  typedef __attribute__((ext_vector_type(2))) unsigned short u16x2;
  u16x2 m = __builtin_elementwise_max(__builtin_elementwise_max(__builtin_bit_cast(u16x2, c4r.x & 0x7fff7fffu), __builtin_bit_cast(u16x2, c4r.y & 0x7fff7fffu)),
                                      __builtin_elementwise_max(__builtin_bit_cast(u16x2, c4i.x & 0x7fff7fffu), __builtin_bit_cast(u16x2, c4i.y & 0x7fff7fffu)));
  float cmax = __uint_as_float((u32)wave_max_i((int)max((u32)m.x, (u32)m.y)) << 16) * dt;
  int ec = min(max((int)(__float_as_uint(cmax) >> 23), 14), 240);
  SG = __uint_as_float((u32)(267 - ec) << 23);
  F = __uint_as_float((u32)(127 - 23) << 23);
  invKS = __uint_as_float((u32)(ec - 3) << 23);
}
DI float exp_bf(float x) {
  float t = x * 1.4426950408889634f;
  float lo = __builtin_fmaf(x, 1.4426950408889634f, -t);
  lo = __builtin_fmaf(x, 1.925963033500e-08f, lo);
  float e = __builtin_amdgcn_exp2f(t);
  return __builtin_fmaf(e * lo, 0.6931471805599453f, e);
}
DI u32 hmul(u32 a, u32 m, u32 mr) {
  h16x2 av = __builtin_bit_cast(h16x2, a);
  h16x2 t = av.xx * __builtin_bit_cast(h16x2, m);
  return __builtin_bit_cast(u32, __builtin_elementwise_fma(av.yy, __builtin_bit_cast(h16x2, mr), t));
}
DI u32 pkr(cf a) { return pk_f16((f32x2){-a.i, a.r}); }

DI void ln_row(u32x4 xv, int lane, float (&nv)[8], u32x2& stat) {
  float v[8];
#pragma unroll
  for (int i = 0; i < 4; ++i) { v[2 * i] = __uint_as_float(xv[i] << 16); v[2 * i + 1] = __uint_as_float(xv[i] & 0xffff0000u); }
  float sm1 = ((v[0] + v[1]) + (v[2] + v[3])) + ((v[4] + v[5]) + (v[6] + v[7]));
  sm1 = lane < 56 ? sm1 : 0.f;
  const float mean = wave_sum(sm1) * (1.f / D);
  float sq = 0.f;
#pragma unroll
  for (int i = 0; i < 8; ++i) { float dd = v[i] - mean; sq = __builtin_fmaf(dd, dd, sq); }
  sq = lane < 56 ? sq : 0.f;
  const float var = wave_sum(sq) * (1.f / D);
  const float mb = rbf(mean), sd = rbf(__builtin_amdgcn_sqrtf(rbf(rbf(var) + 1e-5f))), rinv = __builtin_amdgcn_rcpf(sd);
#pragma unroll
  for (int i = 0; i < 8; ++i) { float dd = rbf(v[i] - mb); float qv = dd * rinv; float rr = __builtin_fmaf(-qv, sd, dd); nv[i] = rbf(__builtin_fmaf(rr, rinv, qv)); }
  stat = (u32x2){pk_bf16(-mb, -mb), __float_as_uint(rinv)};
}
DI void ln_group_rows(LAS char* sm, u32x4 xr0, u32x4 xr1, int lane, int r, int g, char* UT, char* RST) {
  const __amdgpu_buffer_rsrc_t srs = __builtin_amdgcn_make_buffer_rsrc(RST, 0, L * 8, 0x00020000);
  float n0[8], n1[8]; u32x2 st0, st1;
  ln_row(xr0, lane, n0, st0); ln_row(xr1, lane, n1, st1);
  if (lane == 0) __builtin_amdgcn_raw_buffer_store_b128((u32x4){st0.x, st0.y, st1.x, st1.y}, srs, (u32)((g * 8 + 2 * r) * 8), 0, 16);
  if (lane < 56) {
#pragma unroll
    for (int i = 0; i < 8; ++i) *(LAS u32*)(sm + LN_T + (8 * lane + (i ^ (lane & 7))) * 16 + r * 4) = pk_f16((f32x2){n0[i], n1[i]});
  }
  WAIT_LGKM0();
  lds_set(sm, K1_FL + 32 + r); lds_wait4(sm, K1_FL + 32);
  const __amdgpu_buffer_rsrc_t urs = __builtin_amdgcn_make_buffer_rsrc(UT, 0, 256 * UG, 0x00020000);
#pragma unroll
  for (int i = 0; i < 2; ++i) {
    const int e = (2 * r + i) * 64 + lane;
    if (e < 448) { u32x4 ov = *(LAS u32x4*)(sm + LN_T + (e ^ ((e >> 3) & 7)) * 16); __builtin_amdgcn_raw_buffer_store_b128(ov, urs, (u32)(g * UG + e * 16), 0, 16); }
  }
}
DI void ln_publish(LAS char* sm, int lane, u32* fl, u32 tag) {
  u32 old = 0;
  if (lane == 0) old = __hip_atomic_fetch_add((LAS u32*)(sm + K1_FL + 52), 1u, __ATOMIC_RELAXED, __HIP_MEMORY_SCOPE_WORKGROUP);
  if (lane == 0 && old == 3) __hip_atomic_store((gu32*)fl, tag, RLX_AGENT);
}

DI void m_role(LAS char* sm, int wv, int lane, int pm, int pn, int pm2, int pc, int rot1, int rot2, u32& tag, const u16* x, const u16* w1, char* UT, char* RST, char* HF, const u16* w2, u32* flags DBG_ARG) {
  STAMP(0);
  const int r32 = lane & 31, h = lane >> 5;
  LAS char* xr = sm + G1_X + wv * 4096 + r32 * 128;
  const int xsw = (r32 >> 1) & 7;
  LAS char* xrs[4];
#pragma unroll
  for (int s = 0; s < 4; ++s) xrs[s] = xr + (((2 * s + h) ^ xsw) << 4);
  const int q = (lane & 15) >> 2, p = lane & 3, blk = (lane >> 4) & 1;
  LAS char* wbs[4];
#pragma unroll
  for (int c = 0; c < 4; ++c) wbs[c] = sm + G1_W + (8 * h + q) * 256 + ((((c + wv) & 3) ^ q) << 6) + blk * 32 + p * 8;
  struct Frag { s16x4 a[4][2]; };
  auto rd = [&](int s, Frag& f) {
#pragma unroll
    for (int c = 0; c < 4; ++c) {
      f.a[c][0] = __builtin_amdgcn_ds_read_tr16_b64_v4i16((LAS s16x4*)(wbs[c] + s * 4096));
      f.a[c][1] = __builtin_amdgcn_ds_read_tr16_b64_v4i16((LAS s16x4*)(wbs[c] + s * 4096 + 1024));
    }
  };
  f32x16 acc[4] = {};
  f32x16 csa = {};
  const s16x8 ones = {0x3f80, 0x3f80, 0x3f80, 0x3f80, 0x3f80, 0x3f80, 0x3f80, 0x3f80};
  Frag fA, fB;
  const u32 smb1 = (u32)(size_t)sm;
  u32 xoff[4];
#pragma unroll
  for (int i = 0; i < 4; ++i) { int r = 8 * i + (lane >> 3); xoff[i] = (u32)((pm * 128 + wv * 32 + r) * (D * 2) + (((lane & 7) ^ ((r >> 1) & 7)) << 4)); }
  auto xdma = [&](int st, int i) { int se = st + rot1; se = se >= 7 ? se - 7 : se; glds16((const char*)x + se * 128, xoff[i], smb1 + G1_X + (st & 1) * 16384 + wv * 4096 + i * 1024); };
  __builtin_amdgcn_s_setprio(2);
#pragma unroll
  for (int i = 0; i < 4; ++i) xdma(0, i);
#pragma unroll
  for (int i = 0; i < 4; ++i) xdma(1, i);
  STAMP(1);
  s16x8 xs[2][4];
  auto xrd = [&](int st) {
#pragma unroll
    for (int s4 = 0; s4 < 4; ++s4) xs[st & 1][s4] = *(LAS s16x8*)(xrs[s4] + (st & 1) * 16384);
  };
  WAIT_VM(4);
  xrd(0);
  WAIT_LGKM0();
#pragma unroll
  for (int i = 0; i < 4; ++i) xdma(2, i);
  u32 raw = epoch_load(flags);
  lds_wait4(sm, K1_FL + 128);
  STAMP(2);
  rd(0, fA);
  u32 pre = 0;
  auto step = [&](int s, Frag& cur, Frag& nxt) {
    const int st = s >> 2, s4 = s & 3;
    if (s4 == 0 && st >= 1) lds_set(sm, K1_FL + 192 + 4 * (st - 1) + wv);
    if (s4 == 2 && st < 6) {
      if (st <= 4) WAIT_VM(4); else { WAIT_VM(0); TAG_READY(raw, tag); }
      xrd(st + 1);
      pre = flag_pre(sm, K1_FL + 128 + 4 * (st + 1));
    }
    if (s4 == 3 && st < 6) flag_chk(pre, sm, K1_FL + 128 + 4 * (st + 1));
    if (s < 27) rd(s + 1, nxt);
    bf16x8 bx = __builtin_bit_cast(bf16x8, xs[st & 1][s4]);
#define MM(c) acc[c] = __builtin_amdgcn_mfma_f32_32x32x16_bf16(__builtin_bit_cast(bf16x8, __builtin_shufflevector(cur.a[c][0], cur.a[c][1], 0, 1, 2, 3, 4, 5, 6, 7)), bx, acc[c], 0, 0, 0)
    MM(0); MM(1); MM(2);
    __builtin_amdgcn_sched_group_barrier(0x8, 1, 0); __builtin_amdgcn_sched_group_barrier(0x100, 4, 0);
    __builtin_amdgcn_sched_group_barrier(0x8, 1, 0); __builtin_amdgcn_sched_group_barrier(0x100, 4, 0);
    __builtin_amdgcn_sched_group_barrier(0x8, 1, 0); __builtin_amdgcn_sched_group_barrier(0x100, 4, 0);
    __builtin_amdgcn_sched_barrier(0);
    if (s4 == 3 && st + 3 <= 6) { WAIT_LGKM0(); xdma(st + 3, 0); }
    if (s4 == 0 && st >= 1 && st + 2 <= 6) xdma(st + 2, 2);
    MM(3);
    __builtin_amdgcn_sched_barrier(0);
    if (s4 == 3 && st + 3 <= 6) xdma(st + 3, 1);
    if (s4 == 0 && st >= 1 && st + 2 <= 6) xdma(st + 2, 3);
    csa = __builtin_amdgcn_mfma_f32_32x32x16_bf16(__builtin_bit_cast(bf16x8, ones), __builtin_bit_cast(bf16x8, __builtin_shufflevector(cur.a[0][0], cur.a[0][1], 0, 1, 2, 3, 4, 5, 6, 7)), csa, 0, 0, 0);
#undef MM
    __builtin_amdgcn_sched_barrier(0);
  };
#pragma unroll
  for (int s = 0; s < 28; s += 2) {
    if ((s & 3) == 0) STAMP(5 + (s >> 2));
    step(s, fA, fB);
    step(s + 1, fB, fA);
  }
  STAMP(12);
  {
    float c0 = csa[0];
    float p0 = rbf(c0), r1 = c0 - p0, p1 = rbf(r1), p2 = r1 - p1;
    if (h == 0) *(LAS u32x2*)(sm + G1_CS + (32 * wv + r32) * 8) = (u32x2){pk_bf16(p0, p1), pk_bf16(p2, 0.f)};
  }
  WAIT_LGKM0();
  lds_set(sm, K1_FL + 180 + wv);
  lds_wait4(sm, K1_FL + 180); lds_wait4(sm, K1_FL + 176);
  STAMP(13);
  {
    u32 nmb = *(LAS u32*)(sm + G1_RS + (32 * wv + r32) * 8);
    u32x2 cp[4];
#pragma unroll
    for (int c = 0; c < 4; ++c) cp[c] = *(LAS u32x2*)(sm + G1_CS + (32 * ((c + wv) & 3) + r32) * 8);
    nmb = h ? 0u : nmb;
    u32x4 bm = {nmb, nmb, 0u, 0u};
#pragma unroll
    for (int c = 0; c < 4; ++c) {
      u32x4 am = {cp[c].x, cp[c].y, 0u, 0u};
      acc[c] = __builtin_amdgcn_mfma_f32_32x32x16_bf16(__builtin_bit_cast(bf16x8, am), __builtin_bit_cast(bf16x8, bm), acc[c], 0, 0, 0);
    }
    const __amdgpu_buffer_rsrc_t hrs = __builtin_amdgcn_make_buffer_rsrc(HF, 0, L * FF * 2, 0x00020000);
    const u32 ho = (u32)(((pm * 4 + wv) * 112 + pn * 8) * 1024 + lane * 16);
#pragma unroll
    for (int c = 0; c < 4; ++c) {
      const int ct = (c + wv) & 3;
#pragma unroll
      for (int s2 = 0; s2 < 2; ++s2) {
        typedef __attribute__((ext_vector_type(2))) short s16x2;
        const s16x2 zz = {0, 0};
        u32x4 o;
#pragma unroll
        for (int i = 0; i < 4; ++i)
          o[i] = __builtin_bit_cast(u32, __builtin_elementwise_max(__builtin_bit_cast(s16x2, pk_bf16(acc[c][8 * s2 + 2 * i], acc[c][8 * s2 + 2 * i + 1])), zz));
        __builtin_amdgcn_raw_buffer_store_b128(o, hrs, ho + (2 * ct + s2) * 1024, 0, 16);
      }
    }
  }
  STAMP(14);
  __builtin_amdgcn_s_setprio(0);
  const u32 smA = (u32)(size_t)sm + wv * 8192, smB = (u32)(size_t)sm + 32768 + wv * 16384 - 8192;
  const u32 woff = (u32)((wv * 448 + (lane >> 3)) * (D * 2) + pc * 128 + (((lane & 7) ^ (((lane >> 4) & 1) << 2)) << 4));
  auto wdma = [&](int t) {
    int te = t + rot2; te = te >= 14 ? te - 14 : te;
#pragma unroll
    for (int i = 0; i < 4; ++i) glds16((const char*)w2 + (te * 32 + i * 8) * (D * 2), woff, ((t % 6) < 2 ? smA : smB) + (t % 6) * 4096 + i * 1024);
  };
  WAIT_VM(0);
  if (lane == 0) __hip_atomic_store((gu32*)(flags + FL_H + ((pm * 4 + wv) * 16 + pn) * FSP), tag, RLX_AGENT);
  lds_wait4(sm, K1_FL + 64); lds_wait4(sm, K1_FL + 68);
  wdma(0); wdma(1); wdma(2); wdma(3); wdma(4); wdma(5);
  STAMP(15);
  poll_flags(flags + FL_H + ((2 * pm2 + (lane & 1)) * 16 + ((7 * wv) >> 1) + ((lane >> 1) & 3)) * FSP, tag);
  STAMP(16);
  constexpr int PH = 3, NB = PH + 1;
  const char* hb = HF + ((size_t)(pm2 * 2) * 112 + wv * 28) * 1024;
  const u32 lo = lane * 16;
  u32x4 hf[NB][4];
  auto hld = [&](int t, u32x4 (&d)[4]) {
    int te = t + rot2; te = te >= 14 ? te - 14 : te;
    const char* hp = hb + te * 2048;
    asm volatile("global_load_dwordx4 %0, %4, %5 sc1\n\tglobal_load_dwordx4 %1, %4, %6 sc1\n\tglobal_load_dwordx4 %2, %4, %5 offset:1024 sc1\n\tglobal_load_dwordx4 %3, %4, %6 offset:1024 sc1"
                 : "=&v"(d[0]), "=&v"(d[1]), "=&v"(d[2]), "=&v"(d[3]) : "v"(lo), "s"(hp), "s"(hp + 112 * 1024) : "memory");
  };
#pragma unroll
  for (int t = 0; t < PH; ++t) hld(t, hf[t]);
  LAS char* wbA[2]; LAS char* wbB[2];
#pragma unroll
  for (int c = 0; c < 2; ++c) { const u32 lo_ = (4 * h + q) * 128 + ((c ^ ((q >> 1) & 1)) << 6) + blk * 32 + p * 8; wbA[c] = sm + wv * 8192 + lo_; wbB[c] = sm + 32768 + wv * 16384 - 8192 + lo_; }
  f32x16 ac2[2][2] = {};
#pragma unroll
  for (int t = 0; t < 14; ++t) {
    int allow = 0;
    if (t < PH) { allow = 4 * (PH - 1 - t); for (int i = 0; i < t; ++i) allow += ((i + PH < 14) ? 4 : 0) + ((i >= 1 && i + 5 < 14) ? 4 : 0); }
    else { const int i0 = t - PH; allow = (i0 >= 1 && i0 + 5 < 14) ? 4 : 0; for (int i = i0 + 1; i < t; ++i) allow += ((i + PH < 14) ? 4 : 0) + ((i >= 1 && i + 5 < 14) ? 4 : 0); }
    u32x4 (&d)[4] = hf[t % NB];
    asm volatile("s_waitcnt vmcnt(%4)" : "+v"(d[0]), "+v"(d[1]), "+v"(d[2]), "+v"(d[3]) : "i"(allow) : "memory");
    s16x4 wf[2][2][2];
#pragma unroll
    for (int s2 = 0; s2 < 2; ++s2)
#pragma unroll
      for (int c = 0; c < 2; ++c) {
        wf[s2][c][0] = __builtin_amdgcn_ds_read_tr16_b64_v4i16((LAS s16x4*)(((t % 6) < 2 ? wbA[c] : wbB[c]) + (t % 6) * 4096 + s2 * 2048));
        wf[s2][c][1] = __builtin_amdgcn_ds_read_tr16_b64_v4i16((LAS s16x4*)(((t % 6) < 2 ? wbA[c] : wbB[c]) + (t % 6) * 4096 + s2 * 2048 + 1024));
      }
    __builtin_amdgcn_sched_barrier(0);
    if (t + PH < 14) hld(t + PH, hf[(t + PH) % NB]);
    __builtin_amdgcn_sched_barrier(0);
#pragma unroll
    for (int s2 = 0; s2 < 2; ++s2) {
#pragma unroll
      for (int c = 0; c < 2; ++c)
#pragma unroll
        for (int rt = 0; rt < 2; ++rt)
          ac2[c][rt] = __builtin_amdgcn_mfma_f32_32x32x16_bf16(__builtin_bit_cast(bf16x8, __builtin_shufflevector(wf[s2][c][0], wf[s2][c][1], 0, 1, 2, 3, 4, 5, 6, 7)),
                                                               __builtin_bit_cast(bf16x8, d[2 * s2 + rt]), ac2[c][rt], 0, 0, 0);
      __builtin_amdgcn_sched_barrier(0);
      if (s2 == 0 && t >= 1 && t + 5 < 14) wdma(t + 5);
      __builtin_amdgcn_sched_barrier(0);
    }
  }
  STAMP(17);
#pragma unroll
  for (int rt = 0; rt < 2; ++rt) {
    const int row = 32 * rt + r32;
    LAS char* rg = sm + (row < 30 ? wv * 8192 + row * 272 : 32768 + wv * 16384 + (row - 30) * 272) + 16 * h;
#pragma unroll
    for (int c = 0; c < 2; ++c)
#pragma unroll
      for (int g = 0; g < 4; ++g)
        *(LAS f32x4*)(rg + (32 * c + 8 * g) * 4) = (f32x4){ac2[c][rt][4 * g], ac2[c][rt][4 * g + 1], ac2[c][rt][4 * g + 2], ac2[c][rt][4 * g + 3]};
  }
  STAMP(18); RSTAMP(29);
}

DI float swap32_sum(float x) { auto r = __builtin_amdgcn_permlane32_swap(__float_as_uint(x), __float_as_uint(x), false, false); return __uint_as_float(r[0]) + __uint_as_float(r[1]); }
DI float swap16_sum(float x) { auto r = __builtin_amdgcn_permlane16_swap(__float_as_uint(x), __float_as_uint(x), false, false); return __uint_as_float(r[0]) + __uint_as_float(r[1]); }
struct SsmIn { u32 ldt, dsk, la, ai, cr, ci; u32x2 c4r, c4i; u32x4 lut0; u32x2 lut1; };
DI void ssm_load(SsmIn& in, int kw, int lane, int d0, const u16* logA, const u16* Aim, const u16* Cr, const u16* Ci, const u16* logdt, const u16* Dskip) {
  const int ch = kw >> 2, wv = kw & 3, d = d0 + ch;
  const int n = d * NS + wv * 64 + lane;
  in.ldt = logdt[d]; in.la = logA[n]; in.ai = Aim[n]; in.cr = Cr[n]; in.ci = Ci[n];
  in.c4r = *(const u32x2*)(Cr + d * NS + 4 * lane); in.c4i = *(const u32x2*)(Ci + d * NS + 4 * lane);
  in.dsk = Dskip[d];
  const u16* lp = &g_lut.v[ch][wv * 64 + lane][0];
  in.lut0 = *(const u32x4*)lp; in.lut1 = *(const u32x2*)(lp + 8);
}
#define DSR64(dst, base, off) asm volatile("ds_read_b64 %0, %1 offset:%2" : "=v"(dst) : "v"(base), "i"(off) : "memory")
#define DSR128(dst, base, off) asm volatile("ds_read_b128 %0, %1 offset:%2" : "=v"(dst) : "v"(base), "i"(off) : "memory")

DI void h_role(LAS char* sm, int kw, int lane, int bid, int pm, int pn, int pm2, int rot1, u32& tag, const u16* x, const u16* w1,
               const u16* logA, const u16* Aim, const u16* Cr, const u16* Ci, const u16* logdt, const u16* Dskip,
               char* UT, char* RST, u16* ST, u32* flags DBG_ARG) {
  STAMP(0);
  const int d0 = 2 * bid;
  const int wv = kw & 3, ch = kw >> 2, t = wv * 64 + lane;
  const int r32 = lane & 31, h = lane >> 5;
  const u32 smb = (u32)(size_t)sm;
  SsmIn in;
  u32 raw;
  if (kw >= 6) {
    __builtin_amdgcn_s_setprio(1);
    const u32 woff = (u32)((32 * (kw - 6) + (lane >> 4)) * (FF * 2) + pn * 256 + (((lane & 15) ^ (((lane >> 4) & 3) << 2)) << 4));
#pragma unroll
    for (int st = 0; st < 7; ++st) {
      int se = st + rot1; se = se >= 7 ? se - 7 : se;
#pragma unroll
      for (int i = 0; i < 8; ++i) glds16((const char*)w1 + (se * 64 + 4 * i) * (FF * 2), woff, smb + G1_W + st * 16384 + (kw - 6) * 8192 + i * 1024);
      if (st == 1) { WAIT_VM(8); *(volatile LAS u16*)(sm + K1_FL + 128 + 2 * (kw - 6)) = 0x0101; }
      if (st == 3) { WAIT_VM(16); *(volatile LAS u16*)(sm + K1_FL + 128 + 4 + 2 * (kw - 6)) = 0x0101; }
      if (st >= 5) { WAIT_VM(24); *(volatile LAS u16*)(sm + K1_FL + 128 + 4 * (st - 3) + 2 * (kw - 6)) = 0x0101; }
    }
    WAIT_VM(16); *(volatile LAS u16*)(sm + K1_FL + 128 + 4 * 4 + 2 * (kw - 6)) = 0x0101;
    WAIT_VM(8); *(volatile LAS u16*)(sm + K1_FL + 128 + 4 * 5 + 2 * (kw - 6)) = 0x0101;
    WAIT_VM(0); *(volatile LAS u16*)(sm + K1_FL + 128 + 4 * 6 + 2 * (kw - 6)) = 0x0101;
    __builtin_amdgcn_s_setprio(0);
    raw = epoch_load(flags);
    ssm_load(in, kw, lane, d0, logA, Aim, Cr, Ci, logdt, Dskip);
  } else {
    __builtin_amdgcn_s_setprio(3);
    u32x4 xrow = *(const u32x4*)(x + (size_t)((pm * 16 + pn) * 8 + 2 * (kw & 3)) * D + 8 * min(lane, 55));
    u32x4 xrow1 = *(const u32x4*)(x + (size_t)((pm * 16 + pn) * 8 + 2 * (kw & 3) + 1) * D + 8 * min(lane, 55));
    ssm_load(in, kw, lane, d0, logA, Aim, Cr, Ci, logdt, Dskip);
    raw = epoch_load(flags);
    if (kw < 4) ln_group_rows(sm, xrow, xrow1, lane, kw, pm * 16 + pn, UT, RST);
    __builtin_amdgcn_s_setprio(0);
  }
  STAMP(4);
#define SEG() __builtin_amdgcn_sched_barrier(0)
  const float dt = rbf(exp_bf(__uint_as_float(in.ldt << 16)));
  float SG, F, invK;
  kgen_scales(dt, in.c4r, in.c4i, SG, F, invK);
  u32 o[53];
  const float zr = -rbf(exp_bf(__uint_as_float(in.la << 16))) * dt, zi = __uint_as_float(in.ai << 16) * dt;
  cf E8 = cexp1(zr, zi, 8.f), e1 = cexp1(zr, -zi, 1.f), e256 = cexp1(zr, -zi, 256.f);
  cm m8 = mk(E8);
  cf E16 = cmul1(E8, m8); cm m16 = mk(E16);
  cf E24 = cmul1(E16, m8), E32 = cmul1(E16, m16); cm m32 = mk(E32);
  cm m64 = mk(cmul1(E32, m32));
  const u32 p8 = pkc(E8), r8 = pkr(E8), p16 = pkc(E16), r16 = pkr(E16), p24 = pkc(E24), r24 = pkr(E24);
  cf A0 = {16384.f, 0.f}, A1 = {E32.r * 16384.f, E32.i * 16384.f};
  auto hrows = [&](int i) {
    const u32 a0 = pkc(A0), a1 = pkc(A1);
    o[8 * i + 0] = a0; o[8 * i + 1] = hmul(a0, p8, r8); o[8 * i + 2] = hmul(a0, p16, r16); o[8 * i + 3] = hmul(a0, p24, r24);
    o[8 * i + 4] = a1; o[8 * i + 5] = hmul(a1, p8, r8); o[8 * i + 6] = hmul(a1, p16, r16); o[8 * i + 7] = hmul(a1, p24, r24);
    if (i < 3) { A0 = cmul1(A0, m64); A1 = cmul1(A1, m64); }
  };
  hrows(0); hrows(1);
  SEG();
  WAIT_VM(0); TAG_READY(raw, tag);
  if (kw < 4) { ln_publish(sm, lane, flags + FL_U + (pm * 16 + pn) * FSP, tag); }
  STAMP(1);
  SEG();
  hrows(2); hrows(3);
  cm m1 = mk(e1), m256 = mk(e256);
  cf e2 = cmul1(e1, m1); cm m2 = mk(e2);
  cf e3 = cmul1(e2, m1), e4 = cmul1(e2, m2);
  const u32 q1 = pkc(e1), s1 = pkr(e1), q2 = pkc(e2), s2 = pkr(e2), q3 = pkc(e3), s3 = pkr(e3);
  cf g0 = {__uint_as_float(in.cr << 16) * dt * SG, -(__uint_as_float(in.ci << 16) * dt) * SG};
  cf g4 = cmul1(g0, mk(e4));
  u32 b0 = pkc(g0), b4 = pkc(g4);
  o[32 + 0] = b0; o[32 + 1] = hmul(b0, q1, s1); o[32 + 2] = hmul(b0, q2, s2); o[32 + 3] = hmul(b0, q3, s3);
  o[32 + 4] = b4; o[32 + 5] = hmul(b4, q1, s1); o[32 + 6] = hmul(b4, q2, s2); o[32 + 7] = hmul(b4, q3, s3);
  g0 = cmul1(g0, m256); g4 = cmul1(g4, m256);
  b0 = pkc(g0); b4 = pkc(g4);
  o[32 + 8] = hmul(b0, q2, s2); o[32 + 9] = b4; o[32 + 10] = hmul(b4, q2, s2); o[32 + 11] = b0;
  g0 = cmul1(g0, m256); g4 = cmul1(g4, m256);
  o[32 + 12] = pkc(g0); o[32 + 19] = pkc(g4);
  g0 = cmul1(g0, m256); g4 = cmul1(g4, m256);
  o[32 + 13] = pkc(g0); o[32 + 20] = pkc(g4);
#pragma unroll
  for (int k = 4; k <= 8; ++k) { g0 = cmul1(g0, m256); o[42 + k] = pkc(g0); }
  f32x16 tacc = {};
  LAS char* kg = sm + G1_W + (kw < 6 ? kw : kw - 6) * 16384;
  auto round = [&]() {
    LAS u32* hw = (LAS u32*)(kg + lane * 4);
#pragma unroll
    for (int i = 0; i < 53; ++i) hw[i * (KG_ROW / 4)] = o[i];
    LAS char* pa = kg + (32 + min(r32, 20)) * KG_ROW + h * 16;
    LAS char* pb = kg + r32 * KG_ROW + h * 16;
    h16x8 fa[8], fb[8];
#pragma unroll
    for (int ks = 0; ks < 8; ++ks) { fa[ks] = *(LAS h16x8*)(pa + ks * 32); fb[ks] = *(LAS h16x8*)(pb + ks * 32); }
#pragma unroll
    for (int ks = 0; ks < 8; ++ks) tacc = __builtin_amdgcn_mfma_f32_32x32x16_f16(fa[ks], fb[ks], tacc, 0, 0, 0);
    WAIT_LGKM0();
  };
  asm volatile("" : "+v"(o[0]), "+v"(o[1]), "+v"(o[2]), "+v"(o[3]), "+v"(o[4]), "+v"(o[5]), "+v"(o[6]), "+v"(o[7]), "+v"(o[8]), "+v"(o[9]), "+v"(o[10]), "+v"(o[11]), "+v"(o[12]), "+v"(o[13]), "+v"(o[14]), "+v"(o[15]), "+v"(o[16]), "+v"(o[17]), "+v"(o[18]), "+v"(o[19]), "+v"(o[20]), "+v"(o[21]), "+v"(o[22]), "+v"(o[23]), "+v"(o[24]), "+v"(o[25]), "+v"(o[26]));
  asm volatile("" : "+v"(o[27]), "+v"(o[28]), "+v"(o[29]), "+v"(o[30]), "+v"(o[31]), "+v"(o[32]), "+v"(o[33]), "+v"(o[34]), "+v"(o[35]), "+v"(o[36]), "+v"(o[37]), "+v"(o[38]), "+v"(o[39]), "+v"(o[40]), "+v"(o[41]), "+v"(o[42]), "+v"(o[43]), "+v"(o[44]), "+v"(o[45]), "+v"(o[46]), "+v"(o[47]), "+v"(o[48]), "+v"(o[49]), "+v"(o[50]), "+v"(o[51]), "+v"(o[52]));
  STAMP(2);
  lds_wait4(sm, K1_FL + (kw < 6 ? 192 + 4 * kw : 216 + 4 * (kw - 6)));
  round();
  if (kw < 2) *(volatile LAS u32*)(sm + K1_FL + 216 + 4 * kw) = 0x01010101u;
  STAMP(5);
  if (kw == 0) {
    __builtin_amdgcn_s_setprio(3);
    poll_flags(flags + FL_U + (pm * 16 + (lane & 15)) * FSP, tag);
    const __amdgpu_buffer_rsrc_t srs = __builtin_amdgcn_make_buffer_rsrc(RST, 0, L * 8, 0x00020000);
    u32x4 sv = __builtin_amdgcn_raw_buffer_load_b128(srs, (u32)((pm * 128 + 2 * lane) * 8), 0, 16);
    *(LAS u32x4*)(sm + G1_RS + lane * 16) = sv;
    WAIT_LGKM0();
    *(volatile LAS u32*)(sm + K1_FL + 176) = 0x01010101u;
    __builtin_amdgcn_s_setprio(0);
  }
  if (kw >= 1 && kw <= 4) { __builtin_amdgcn_s_setprio(3); poll_flags(flags + FL_U + ((kw - 1) * 64 + lane) * FSP, tag); __builtin_amdgcn_s_setprio(0); }
  lds_wait4(sm, K1_FL + 180);
  __builtin_amdgcn_s_setprio(1);
  STAMP(3);
#pragma unroll
  for (int i = 0; i < 12; ++i) {
    const int a = (i & 3) + 8 * (i >> 2) + 4 * h;
    if (a < 22) *(LAS float*)(sm + K2_PT + kw * 2816 + (a * 32 + r32) * 4) = tacc[i];
  }
  STAMP(8);
  lds_bar8(sm, K1_FL + 64, kw);
  STAMP(9);
  u32x4 uv;
  {
    __builtin_amdgcn_s_setprio(3);
    const __amdgpu_buffer_rsrc_t urs = __builtin_amdgcn_make_buffer_rsrc(UT, 0, 256 * UG, 0x00020000);
    uv = __builtin_amdgcn_raw_buffer_load_b128(urs, (u32)((4 * lane + wv) * UG + (d0 + ch) * 16), 0, 16);
    __builtin_amdgcn_s_setprio(1);
  }
  {
    LAS char* pb0 = sm + K2_PT + ch * 4 * 2816 + t * 16;
    const u32 st = 2816;
    f32x4 v = {0.f, 0.f, 0.f, 0.f};
    if (t < 176) v = ((*(LAS f32x4*)(pb0) + *(LAS f32x4*)(pb0 + st)) + (*(LAS f32x4*)(pb0 + 2 * st) + *(LAS f32x4*)(pb0 + 3 * st))) * F;
    if (t < 184) *(LAS f32x4*)(sm + K2_T + ch * 2944 + t * 16) = v;
  }
  STAMP(10);
  lds_bar8(sm, K1_FL + 72, kw);
  STAMP(11);
  {
    float v[11];
#pragma unroll
    for (int e = 0; e < 11; ++e) {
      u32 w = e < 8 ? in.lut0[e >> 1] : in.lut1[(e - 8) >> 1];
      u32 off = (e & 1) ? (w >> 16) : (w & 0xffffu);
      v[e] = *(LAS float*)(sm + K2_T + off);
    }
    u32 ev[5], od[5];
#pragma unroll
    for (int i = 0; i < 5; ++i) { ev[i] = pk_f16((f32x2){v[2 * i], v[2 * i + 1]}); od[i] = pk_f16((f32x2){v[2 * i + 1], v[2 * i + 2]}); }
    LAS char* kt = sm + CV_K + ch * KT_BYTES + t * 16;
    *(LAS u32x4*)(kt + 3 * KT_COPY) = (u32x4){ev[0], ev[1], ev[2], ev[3]};
    *(LAS u32x4*)(kt + 2 * KT_COPY) = (u32x4){od[0], od[1], od[2], od[3]};
    *(LAS u32x4*)(kt + 1 * KT_COPY) = (u32x4){ev[1], ev[2], ev[3], ev[4]};
    *(LAS u32x4*)(kt + 0 * KT_COPY) = (u32x4){od[1], od[2], od[3], od[4]};
    if (lane < 32) *(LAS u32x4*)(sm + CV_U + ch * 6144 + wv * 1536 + lane * 16) = (u32x4){0u, 0u, 0u, 0u};
    if (t < 16) *(LAS u32x4*)(sm + CV_K + ch * KT_BYTES + (t >> 2) * KT_COPY + 4096 + (t & 3) * 16) = (u32x4){0u, 0u, 0u, 0u};
  }
  STAMP(12);
  *(LAS u32x4*)(sm + CV_U + ch * 6144 + wv * 1536 + 512 + lane * 16) = uv;
  lds_bar8(sm, K1_FL + 80, kw);
  STAMP(13);
  f32x16 accA = {}, accB = {};
  {
    const u32 ka = smb + CV_K + ch * KT_BYTES + (r32 & 3) * KT_COPY + (2044 + 8 * h - 4 * (r32 >> 2)) * 2 - 7 * 64;
    const u32 ub = smb + CV_U + ch * 6144 + h * 1536 + (32 + r32) * 16 - 7 * 16;
    u32 kb[3], uu[3];
#pragma unroll
    for (int i = 0; i < 3; ++i) { const int sg = 3 * wv + i, g = sg >= 4, dl0 = 8 * (g ? sg - 4 : sg); kb[i] = ka - dl0 * 64; uu[i] = ub + (32 * g - dl0) * 16; }
    u32x2 a0[2][4], a1[2][4]; u32x4 bb[2][4];
#define CV_RD(n, buf, m) do { DSR64(a0[buf][m], kb[(n) >> 2], (7 - 2 * ((n) & 3) - ((m) >> 1)) * 64 + ((m) & 1) * 32); \
                              DSR64(a1[buf][m], kb[(n) >> 2], (7 - 2 * ((n) & 3) - ((m) >> 1)) * 64 + ((m) & 1) * 32 + 8); \
                              DSR128(bb[buf][m], uu[(n) >> 2], (7 - 2 * ((n) & 3) - ((m) >> 1)) * 16 + ((m) & 1) * 3072); } while (0)
#define CV_WAIT(buf, cnt) asm volatile("s_waitcnt lgkmcnt(" #cnt ")" : "+v"(a0[buf][0]), "+v"(a0[buf][1]), "+v"(a0[buf][2]), "+v"(a0[buf][3]), "+v"(a1[buf][0]), "+v"(a1[buf][1]), "+v"(a1[buf][2]), "+v"(a1[buf][3]), \
                                       "+v"(bb[buf][0]), "+v"(bb[buf][1]), "+v"(bb[buf][2]), "+v"(bb[buf][3]) :: "memory")
#define CV_MM(acc, buf, m) acc = __builtin_amdgcn_mfma_f32_32x32x16_f16(__builtin_bit_cast(h16x8, (u32x4){a0[buf][m].x, a0[buf][m].y, a1[buf][m].x, a1[buf][m].y}), __builtin_bit_cast(h16x8, bb[buf][m]), acc, 0, 0, 0)
#define CV_BATCH(n, acc) do { CV_WAIT((n) & 1, 0); \
      CV_MM(acc, (n) & 1, 0); __builtin_amdgcn_sched_barrier(0); if ((n) < 11) { CV_RD((n) + 1, ((n) + 1) & 1, 0); CV_RD((n) + 1, ((n) + 1) & 1, 1); CV_RD((n) + 1, ((n) + 1) & 1, 2); CV_RD((n) + 1, ((n) + 1) & 1, 3); } __builtin_amdgcn_sched_barrier(0); \
      CV_MM(acc, (n) & 1, 1); CV_MM(acc, (n) & 1, 2); CV_MM(acc, (n) & 1, 3); __builtin_amdgcn_sched_barrier(0); } while (0)
    CV_RD(0, 0, 0); CV_RD(0, 0, 1); CV_RD(0, 0, 2); CV_RD(0, 0, 3);
    CV_BATCH(0, accA); CV_BATCH(1, accA); CV_BATCH(2, accA); CV_BATCH(3, accA);
    CV_BATCH(4, accB); CV_BATCH(5, accB); CV_BATCH(6, accB); CV_BATCH(7, accB);
    CV_BATCH(8, accB); CV_BATCH(9, accB); CV_BATCH(10, accB); CV_BATCH(11, accB);
  }
  STAMP(14);
  lds_bar8(sm, K1_FL + 96, kw);
  {
    const int slot = wv == 0 ? 0 : wv + 1;
    LAS char* pp = sm + CV_P + ch * 20480 + slot * 4096 + r32 * 128;
    if (wv == 1) {
#pragma unroll
      for (int g = 0; g < 4; ++g) *(LAS f32x4*)(pp - 4096 + (((2 * g + h) ^ (r32 & 7)) << 4)) = (f32x4){accA[4 * g], accA[4 * g + 1], accA[4 * g + 2], accA[4 * g + 3]};
    } else {
      accB += accA;
    }
#pragma unroll
    for (int g = 0; g < 4; ++g) *(LAS f32x4*)(pp + (((2 * g + h) ^ (r32 & 7)) << 4)) = (f32x4){accB[4 * g], accB[4 * g + 1], accB[4 * g + 2], accB[4 * g + 3]};
  }
  lds_bar8(sm, K1_FL + 104, kw);
  STAMP(15);
  {
    const int a = t >> 2, bq = t & 3, g = a >> 5, ar = a & 31;
    LAS char* pp = sm + CV_P + ch * 20480 + (g ? 2 * 4096 : 0) + ar * 128;
    const u32 o0 = ((2 * bq) ^ (ar & 7)) << 4, o1 = ((2 * bq + 1) ^ (ar & 7)) << 4;
    f32x4 y0 = *(LAS f32x4*)(pp + o0) + *(LAS f32x4*)(pp + 4096 + o0);
    f32x4 y1 = *(LAS f32x4*)(pp + o1) + *(LAS f32x4*)(pp + 4096 + o1);
    if (g) { y0 += *(LAS f32x4*)(pp + 8192 + o0); y1 += *(LAS f32x4*)(pp + 8192 + o1); }
    h16x8 u = *(LAS h16x8*)(sm + CV_U + ch * 6144 + bq * 1536 + (32 + a) * 16);
    const float ds = __uint_as_float(in.dsk << 16);
    float r[8];
#pragma unroll
    for (int i = 0; i < 8; ++i) { float y = (i < 4 ? y0[i & 3] : y1[i & 3]) * invK; r[i] = rbf(y) + rbf((float)u[i] * ds); }
    u32x4 ov = {pk_bf16(r[0], r[1]), pk_bf16(r[2], r[3]), pk_bf16(r[4], r[5]), pk_bf16(r[6], r[7])};
    const __amdgpu_buffer_rsrc_t rs = __builtin_amdgcn_make_buffer_rsrc(ST, 0, D * LP * 2, 0x00020000);
    __builtin_amdgcn_s_setprio(3);
    __builtin_amdgcn_raw_buffer_store_b128(ov, rs, (u32)(((d0 + ch) * LP + 8 * t) * 2), 0, 16);
  }
  WAIT_VM(0);
  {
    u32 old = 0;
    if (lane == 0) old = __hip_atomic_fetch_add((LAS u32*)(sm + K1_FL + 88), 1u, __ATOMIC_RELAXED, __HIP_MEMORY_SCOPE_WORKGROUP);
    if (lane == 0 && old == 7) __hip_atomic_store((gu32*)(flags + FL_S + bid * FSP), tag, RLX_AGENT);
  }
  STAMP(16); RSTAMP(29);
}

#define ROT2 ((7 * (idx & 3) / 2 + 2 * (idx >> 2)) % 14)
__global__ __launch_bounds__(768, 3) void p_kernel(const u16* x, const u16* logA, const u16* Aim, const u16* Cr, const u16* Ci, const u16* logdt,
                                                   const u16* Dskip, const u16* w1, const u16* w2, char* ws, u16* out) {
  extern __shared__ __attribute__((aligned(16))) char smem[];
  LAS char* sm = (LAS char*)smem;
  const int tid = threadIdx.x, lane = tid & 63;
  const int wave = __builtin_amdgcn_readfirstlane(tid >> 6);
  const int bid = blockIdx.x;
  RSTAMP(30);
  const int xcd = bid & 7, idx = bid >> 3;
  const int pm = (xcd >> 1) * 4 + (idx & 3), pn = (xcd & 1) * 7 + (idx >> 2);
  const int pm2 = xcd * 4 + (idx & 3), pc = idx >> 2;
  const int rot1 = (2 * (idx & 3) + (idx >> 2)) % 7, rot2 = ROT2;
  u32* flags = (u32*)(ws + WS_FLAG);
  u32 tag = 0;
  if (tid < 64) *(LAS u32*)(sm + K1_FL + tid * 4) = 0u;
  WAIT_LGKM0(); CBAR();
  if (bid >= 224) {
    if (wave < 4) {
      const int e = bid - 224, g = (e >> 1) * 16 + 14 + (e & 1);
      u32x4 xrow = *(const u32x4*)(x + (size_t)(g * 8 + 2 * wave) * D + 8 * min(lane, 55));
      u32x4 xrow1 = *(const u32x4*)(x + (size_t)(g * 8 + 2 * wave + 1) * D + 8 * min(lane, 55));
      tag = epoch_load(flags) + 1u;
      ln_group_rows(sm, xrow, xrow1, lane, wave, g, ws + WS_UT, ws + WS_RINV);
      WAIT_VM(0);
      ln_publish(sm, lane, flags + FL_U + g * FSP, tag);
    }
    {
      const char* wq = (const char*)w2 + ((bid - 224) >> 3) * 401408 + lane * 16;
      u32x4 accw = {0u, 0u, 0u, 0u};
#pragma unroll 1
      for (int i0 = wave; i0 < 392; i0 += 12 * 11) {
        u32x4 v[11];
#pragma unroll
        for (int j = 0; j < 11; ++j) v[j] = *(const u32x4*)(wq + min(i0 + 12 * j, 391) * 1024);
#pragma unroll
        for (int j = 0; j < 11; ++j) accw ^= v[j];
      }
      asm volatile("" :: "v"(accw));
    }
    return;
  }
  if (wave < 4) {
    m_role(sm, wave, lane, pm, pn, pm2, pc, rot1, rot2, tag, x, w1, ws + WS_UT, ws + WS_RINV, ws + WS_H, w2, flags DBG_PASS);
  } else {
    h_role(sm, wave - 4, lane, xcd * 28 + idx, pm, pn, pm2, rot1, tag, x, w1, logA, Aim, Cr, Ci, logdt, Dskip, ws + WS_UT, ws + WS_RINV, (u16*)(ws + WS_ST), flags DBG_PASS);
  }
  const int e = tid - 256, row = (e >> 3) & 63, cq = e & 7;
  u32x4 xv; float rinv = 0.f, sval[8];
  if (wave >= 4) {
    xv = *(const u32x4*)(x + (size_t)(pm2 * 64 + row) * D + pc * 64 + 8 * cq);
    const __amdgpu_buffer_rsrc_t srs = __builtin_amdgcn_make_buffer_rsrc(ws + WS_RINV, 0, L * 8, 0x00020000);
    rinv = __uint_as_float(__builtin_amdgcn_raw_buffer_load_b32(srs, (u32)((pm2 * 64 + row) * 8 + 4), 0, 16));
    if (wave == 4) poll_flags(flags + FL_S + (pc * 32 + (lane & 31)) * FSP, tag);
    lds_bar8s(sm, K1_FL + 120, wave - 4);
    const __amdgpu_buffer_rsrc_t rs = __builtin_amdgcn_make_buffer_rsrc(ws + WS_ST, 0, D * LP * 2, 0x00020000);
    u32x4 sv = __builtin_amdgcn_raw_buffer_load_b128(rs, (u32)(((pc * 64 + (e >> 3)) * LP + pm2 * 64 + 8 * (e & 7)) * 2), 0, 16);
    *(LAS u32x4*)(sm + K2_S + (e >> 3) * 144 + (e & 7) * 16) = sv;
    lds_bar8(sm, K1_FL + 112, wave - 4);
#pragma unroll
    for (int i = 0; i < 8; ++i) sval[i] = bf2f(*(LAS u16*)(sm + K2_S + (8 * cq + i) * 144 + row * 2));
  }
  STAMP(20);
  WAIT_LGKM0(); CBAR();
  STAMP(21);
  if (wave >= 4) {
    f32x4 z0 = {0.f, 0.f, 0.f, 0.f}, z1 = {0.f, 0.f, 0.f, 0.f};
    const u32 ro = (row < 30 ? row * 272 : 32768 + (row - 30) * 272) + cq * 32, rstep = row < 30 ? 8192 : 16384;
#pragma unroll
    for (int w = 0; w < 4; ++w) {
      z0 += *(LAS f32x4*)(sm + ro + w * rstep);
      z1 += *(LAS f32x4*)(sm + ro + w * rstep + 16);
    }
    float r[8];
#pragma unroll
    for (int i = 0; i < 8; ++i) {
      float o = rbf((i < 4 ? z0[i & 3] : z1[i & 3]) * rinv);
      float g = rbf(o * __builtin_amdgcn_rcpf(1.f + __builtin_amdgcn_exp2f(-1.4426950408889634f * o)));
      float xx = (i & 1) ? __uint_as_float(xv[i >> 1] & 0xffff0000u) : __uint_as_float(xv[i >> 1] << 16);
      r[i] = xx + rbf(sval[i] * g);
    }
    *(u32x4*)(out + (size_t)(pm2 * 64 + row) * D + pc * 64 + 8 * cq) = (u32x4){pk_bf16(r[0], r[1]), pk_bf16(r[2], r[3]), pk_bf16(r[4], r[5]), pk_bf16(r[6], r[7])};
  } else if (bid == 0 && wave == 0) {
#pragma unroll 1
    for (int i = 0; i < 4; ++i) poll_flags(flags + FL_S + min(i * 64 + lane, 223) * FSP, tag);
    if (lane == 0) __hip_atomic_store((gu32*)flags, tag, RLX_AGENT);
  }
  STAMP(22);
  STAMP_DUMP();
}
}

#define LDSZ(x) (x)
extern "C" void kernel_launch(void* const* d_in, const int* in_sizes, int n_in,
                              void* d_out, int out_size, void* d_ws, size_t ws_size,
                              hipStream_t stream) {
  (void)hipFuncSetAttribute((const void*)p_kernel, hipFuncAttributeMaxDynamicSharedMemorySize, LDSZ(P_LDS));
  p_kernel<<<256, 768, LDSZ(P_LDS), stream>>>((const u16*)d_in[0], (const u16*)d_in[1], (const u16*)d_in[2], (const u16*)d_in[3], (const u16*)d_in[4], (const u16*)d_in[5],
                                              (const u16*)d_in[6], (const u16*)d_in[8], (const u16*)d_in[9], (char*)d_ws, (u16*)d_out);
}
```

```cpp
#include <hip/hip_runtime.h>
#include <hip/hip_bf16.h>
#include <stdint.h>

namespace {
constexpr int L = 2048, D = 448, NS = 256, FF = 1792;
typedef unsigned short u16;
typedef unsigned int u32;
typedef __attribute__((ext_vector_type(2))) float f32x2;
typedef __attribute__((ext_vector_type(4))) float f32x4;
typedef __attribute__((ext_vector_type(16))) float f32x16;
typedef __attribute__((ext_vector_type(2))) _Float16 h16x2;
typedef __attribute__((ext_vector_type(8))) _Float16 h16x8;
typedef __attribute__((ext_vector_type(2))) __bf16 bf16x2;
typedef __attribute__((ext_vector_type(8))) __bf16 bf16x8;
typedef __attribute__((ext_vector_type(4))) short s16x4;
typedef __attribute__((ext_vector_type(8))) short s16x8;
typedef __attribute__((ext_vector_type(2))) u32 u32x2;
typedef __attribute__((ext_vector_type(4))) u32 u32x4;
#define LAS __attribute__((address_space(3)))
#define DI __device__ __forceinline__

constexpr size_t WS_UT = 0;
constexpr size_t WS_KT = 2u << 20;
constexpr size_t WS_KS = 10u << 20;
constexpr size_t WS_H = 4u << 20;
constexpr size_t WS_ST = 2u << 20;
constexpr size_t WS_FLAG = (2u << 20) - (128u << 10);
constexpr size_t WS_CS = (2u << 20) - (144u << 10);
constexpr size_t WS_RINV = (2u << 20) - (160u << 10);
constexpr size_t WS_W2F = 26u << 20;
constexpr int LP = L + 128;
constexpr int FSP = 16;
constexpr int KT_COPY = 4160;
constexpr int KT_BYTES = 4 * KT_COPY;

DI float bf2f(u16 v) { return __uint_as_float(((u32)v) << 16); }
DI float rbf(float f) { return (float)(__bf16)f; }
DI u32 pk_bf16(float a, float b) { f32x2 v = {a, b}; return __builtin_bit_cast(u32, __builtin_convertvector(v, bf16x2)); }
DI u32 pk_f16(f32x2 v) { return __builtin_bit_cast(u32, __builtin_convertvector(v, h16x2)); }

DI f32x2 cmul(f32x2 a, f32x2 b, f32x2 bs) {
  f32x2 t = a.xx * b;
  return __builtin_elementwise_fma(a.yy, bs, t);
}
DI f32x2 rot(f32x2 b) { f32x2 r = {-b.y, b.x}; return r; }

template <int CTRL> DI int dpp_i(int x) { return __builtin_amdgcn_update_dpp(0, x, CTRL, 0xf, 0xf, false); }
DI float wave_sum(float x) {
  x += __builtin_bit_cast(float, dpp_i<0xB1>(__builtin_bit_cast(int, x)));
  x += __builtin_bit_cast(float, dpp_i<0x4E>(__builtin_bit_cast(int, x)));
  x += __builtin_bit_cast(float, dpp_i<0x141>(__builtin_bit_cast(int, x)));
  x += __builtin_bit_cast(float, dpp_i<0x140>(__builtin_bit_cast(int, x)));
  int xi = __builtin_bit_cast(int, x);
  float a = __builtin_bit_cast(float, __builtin_amdgcn_readlane(xi, 0)), b = __builtin_bit_cast(float, __builtin_amdgcn_readlane(xi, 16));
  float c = __builtin_bit_cast(float, __builtin_amdgcn_readlane(xi, 32)), d = __builtin_bit_cast(float, __builtin_amdgcn_readlane(xi, 48));
  return (a + b) + (c + d);
}
DI int wave_max_i(int x) {
  x = max(x, dpp_i<0xB1>(x)); x = max(x, dpp_i<0x4E>(x)); x = max(x, dpp_i<0x141>(x)); x = max(x, dpp_i<0x140>(x));
  return max(max(__builtin_amdgcn_readlane(x, 0), __builtin_amdgcn_readlane(x, 16)), max(__builtin_amdgcn_readlane(x, 32), __builtin_amdgcn_readlane(x, 48)));
}

DI void glds16(const void* sbase, u32 voff, u32 ldst) {
  u32 keep;
  asm volatile("s_mov_b32 %0, m0\n\ts_mov_b32 m0, %3\n\ts_nop 0\n\tglobal_load_lds_dwordx4 %1, %2\n\ts_mov_b32 m0, %0"
               : "=&s"(keep) : "v"(voff), "s"(sbase), "s"(ldst) : "memory");
}
#define WAIT_VM(n) asm volatile("s_waitcnt vmcnt(" #n ")" ::: "memory")
#define WAIT_LGKM0() asm volatile("s_waitcnt lgkmcnt(0)" ::: "memory")

DI void lds_wait4(LAS char* sm, u32 off) {
  u32 addr = (u32)(size_t)sm + off, tv, td, ts;
  asm volatile("v_mov_b32 %0, %3\n"
               "WL%=:\n\ts_sleep 1\n\tds_read_b32 %1, %0\n\ts_waitcnt lgkmcnt(0)\n\tv_readfirstlane_b32 %2, %1\n\ts_cmp_lg_u32 %2, 0x01010101\n\ts_cbranch_scc1 WL%=\n"
               : "=&v"(tv), "=&v"(td), "=&s"(ts) : "s"(addr) : "memory", "scc");
}
DI u32 flag_pre(LAS char* sm, u32 off) { return *(volatile LAS u32*)(sm + off); }
DI void flag_chk(u32 v, LAS char* sm, u32 off) {
  u32 addr = (u32)(size_t)sm + off, tv, ts;
  asm volatile("v_readfirstlane_b32 %2, %0\n\ts_cmp_eq_u32 %2, 0x01010101\n\ts_cbranch_scc1 FD%=\n\tv_mov_b32 %1, %3\n"
               "FL%=:\n\tds_read_b32 %0, %1\n\ts_waitcnt lgkmcnt(0)\n\tv_readfirstlane_b32 %2, %0\n\ts_cmp_lg_u32 %2, 0x01010101\n\ts_cbranch_scc1 FL%=\nFD%=:\n"
               : "+v"(v), "=&v"(tv), "=&s"(ts) : "s"(addr) : "memory", "scc");
}
DI void lds_set(LAS char* sm, u32 off) {
  *(volatile LAS unsigned char*)(sm + off) = 1;
}
DI void lds_bar4(LAS char* sm, u32 off, u32 target, int lane) {
  WAIT_LGKM0();
  if (lane == 0) __hip_atomic_fetch_add((LAS u32*)(sm + off), 1u, __ATOMIC_RELAXED, __HIP_MEMORY_SCOPE_WORKGROUP);
  for (;;) {
    u32 v = *(volatile LAS u32*)(sm + off);
    if ((u32)__builtin_amdgcn_readfirstlane(v) >= target) break;
  }
  asm volatile("" ::: "memory");
}

constexpr u32 G1_W = 0;
constexpr u32 G1_X = 65536;
constexpr u32 KG_ROW = 272, KG_SZ = 53 * KG_ROW;
constexpr u32 LN_T = 49152;
constexpr u32 SSM0 = 98304;
constexpr u32 G1_CS = 159904;
constexpr u32 G1_RS = G1_CS + 1024;
constexpr u32 K1_FL = G1_RS + 1024;
constexpr u32 P_LDS = K1_FL + 256;
constexpr u32 CV_U = SSM0;
constexpr u32 CV_K = SSM0 + 12288;
constexpr u32 CV_P = CV_K;
constexpr u32 K2_PT = SSM0;
constexpr u32 K2_T = 151552;
constexpr u32 K2_S = SSM0;
constexpr u32 P2_R = 0;
constexpr int UG = 7424;
#define CBAR() do { asm volatile("" ::: "memory"); __builtin_amdgcn_s_barrier(); asm volatile("" ::: "memory"); } while (0)
typedef __attribute__((address_space(1))) u32 gu32;
#define RLX_AGENT __ATOMIC_RELAXED, __HIP_MEMORY_SCOPE_AGENT
constexpr int FL_H = 16 * FSP, FL_U = 1056 * FSP, FL_S = 1328 * FSP, FL_C = 1560 * FSP;
#define PSLEEP 8
DI u32 epoch_load(const u32* flags) { return __hip_atomic_load((const gu32*)flags, RLX_AGENT); }
#define TAG_READY(raw, tag) do { asm volatile("" : "+v"(raw)); tag = raw + 1u; } while (0)
DI void poll_flags(const u32* p, u32 tag) {
  for (u32 spins = 0; spins < (1u << 22); ++spins) {
    u32 v = __hip_atomic_load((const gu32*)p, RLX_AGENT);
    if (__all(v == tag)) break;
    __builtin_amdgcn_s_sleep(PSLEEP);
  }
  asm volatile("" ::: "memory");
}

struct LutT { u16 v[2][256][12]; };
constexpr LutT make_lut() {
  LutT t{};
  for (int ch = 0; ch < 2; ++ch)
    for (int th = 0; th < 256; ++th)
      for (int e = 0; e < 12; ++e) {
        int j = 2047 - 8 * th - e;
        int off = 22 * 128;
        if (j >= 0) {
          int p = j;
          if (j >= 256) { int sh = j >= 1024 ? 3 : (j >= 512 ? 2 : 1); int q = j >> sh, r = j & ((1 << sh) - 1), hf = 1 << (sh - 1); if (r > hf || (r == hf && (q & 1))) ++q; p = q << sh; }
          int b = (p & 255) >> 3, lo = p & 7, k = p >> 8, a = 0;
          if (k == 0) a = lo; else if (lo == 0) a = 10 + k; else if (k == 1) a = 7 + (lo >> 1); else a = 17 + k;
          off = (a * 32 + b) * 4;
        }
        t.v[ch][th][e] = (u16)(off + ch * 2944);
      }
  return t;
}
__device__ const LutT g_lut = make_lut();

DI f32x2 cexp_hw(float zr, float zi, float p) {
  float ph = zi * p;
  float k = __builtin_rintf(ph * 0.15915494309189535f);
  float r = __builtin_fmaf(ph, 0.15915494309189535f, -k);
  r = __builtin_fmaf(ph, 6.4206382433e-09f, r);
  float e = __builtin_amdgcn_exp2f(zr * p * 1.4426950408889634f);
  return (f32x2){e * __builtin_amdgcn_cosf(r), e * __builtin_amdgcn_sinf(r)};
}
DI void lds_bar8(LAS char* sm, u32 off, int kw) {
  WAIT_LGKM0();
  *(volatile LAS unsigned char*)(sm + off + kw) = 1;
  u32 addr = (u32)(size_t)sm + off, tv, td, te, ts;
  asm volatile("v_mov_b32 %0, %4\n"
               "WB%=:\n\ts_sleep 1\n\tds_read_b32 %1, %0\n\tds_read_b32 %2, %0 offset:4\n\ts_waitcnt lgkmcnt(0)\n\tv_and_b32 %1, %1, %2\n\ts_nop 0\n\tv_readfirstlane_b32 %3, %1\n\ts_cmp_lg_u32 %3, 0x01010101\n\ts_cbranch_scc1 WB%=\n"
               : "=&v"(tv), "=&v"(td), "=&v"(te), "=&s"(ts) : "s"(addr) : "memory", "scc");
}
DI void lds_bar8s(LAS char* sm, u32 off, int kw) {
  WAIT_LGKM0();
  *(volatile LAS unsigned char*)(sm + off + kw) = 1;
  u32 addr = (u32)(size_t)sm + off, tv, td, te, ts;
  asm volatile("v_mov_b32 %0, %4\n"
               "WS%=:\n\ts_sleep 2\n\tds_read_b32 %1, %0\n\tds_read_b32 %2, %0 offset:4\n\ts_waitcnt lgkmcnt(0)\n\tv_and_b32 %1, %1, %2\n\ts_nop 0\n\tv_readfirstlane_b32 %3, %1\n\ts_cmp_lg_u32 %3, 0x01010101\n\ts_cbranch_scc1 WS%=\n"
               : "=&v"(tv), "=&v"(td), "=&v"(te), "=&s"(ts) : "s"(addr) : "memory", "scc");
}
struct cf { float r, i; };
DI float amul(float a, float b) { float o; asm("v_mul_f32 %0, %1, %2" : "=v"(o) : "v"(a), "v"(b)); return o; }
DI void afmac(float& d, float a, float b) { asm("v_fmac_f32 %0, %1, %2" : "+v"(d) : "v"(a), "v"(b)); }
struct cm { float r, i, ni; };
DI cm mk(cf e) { return cm{e.r, e.i, -e.i}; }
DI cf cmul1(cf a, cm b) { cf o; o.r = amul(a.r, b.r); o.i = amul(a.r, b.i); afmac(o.r, a.i, b.ni); afmac(o.i, a.i, b.r); return o; }
DI u32 pkc(cf a) { return pk_f16((f32x2){a.r, a.i}); }
DI cf cexp1(float zr, float zi, float p) { f32x2 e = cexp_hw(zr, zi, p); return cf{e.x, e.y}; }

DI void kgen_scales(float dt, u32x2 c4r, u32x2 c4i, float& SG, float& F, float& invKS) {
  typedef __attribute__((ext_vector_type(2))) unsigned short u16x2;
  u16x2 m = __builtin_elementwise_max(__builtin_elementwise_max(__builtin_bit_cast(u16x2, c4r.x & 0x7fff7fffu), __builtin_bit_cast(u16x2, c4r.y & 0x7fff7fffu)),
                                      __builtin_elementwise_max(__builtin_bit_cast(u16x2, c4i.x & 0x7fff7fffu), __builtin_bit_cast(u16x2, c4i.y & 0x7fff7fffu)));
  float cmax = __uint_as_float((u32)wave_max_i((int)max((u32)m.x, (u32)m.y)) << 16) * dt;
  int ec = min(max((int)(__float_as_uint(cmax) >> 23), 14), 240);
  SG = __uint_as_float((u32)(267 - ec) << 23);
  F = __uint_as_float((u32)(127 - 23) << 23);
  invKS = __uint_as_float((u32)(ec - 3) << 23);
}
DI float exp_bf(float x) {
  float t = x * 1.4426950408889634f;
  float lo = __builtin_fmaf(x, 1.4426950408889634f, -t);
  lo = __builtin_fmaf(x, 1.925963033500e-08f, lo);
  float e = __builtin_amdgcn_exp2f(t);
  return __builtin_fmaf(e * lo, 0.6931471805599453f, e);
}
DI u32 hmul(u32 a, u32 m, u32 mr) {
  h16x2 av = __builtin_bit_cast(h16x2, a);
  h16x2 t = av.xx * __builtin_bit_cast(h16x2, m);
  return __builtin_bit_cast(u32, __builtin_elementwise_fma(av.yy, __builtin_bit_cast(h16x2, mr), t));
}
DI u32 pkr(cf a) { return pk_f16((f32x2){-a.i, a.r}); }

DI void ln_row(u32x4 xv, int lane, float (&nv)[8], u32x2& stat) {
  float v[8];
#pragma unroll
  for (int i = 0; i < 4; ++i) { v[2 * i] = __uint_as_float(xv[i] << 16); v[2 * i + 1] = __uint_as_float(xv[i] & 0xffff0000u); }
  float sm1 = ((v[0] + v[1]) + (v[2] + v[3])) + ((v[4] + v[5]) + (v[6] + v[7]));
  sm1 = lane < 56 ? sm1 : 0.f;
  const float mean = wave_sum(sm1) * (1.f / D);
  float sq = 0.f;
#pragma unroll
  for (int i = 0; i < 8; ++i) { float dd = v[i] - mean; sq = __builtin_fmaf(dd, dd, sq); }
  sq = lane < 56 ? sq : 0.f;
  const float var = wave_sum(sq) * (1.f / D);
  const float mb = rbf(mean), sd = rbf(__builtin_amdgcn_sqrtf(rbf(rbf(var) + 1e-5f))), rinv = __builtin_amdgcn_rcpf(sd);
#pragma unroll
  for (int i = 0; i < 8; ++i) { float dd = rbf(v[i] - mb); float qv = dd * rinv; float rr = __builtin_fmaf(-qv, sd, dd); nv[i] = rbf(__builtin_fmaf(rr, rinv, qv)); }
  stat = (u32x2){pk_bf16(-mb, -mb), __float_as_uint(rinv)};
}
DI void ln_group_rows(LAS char* sm, u32x4 xr0, u32x4 xr1, int lane, int r, int g, char* UT, char* RST) {
  const __amdgpu_buffer_rsrc_t srs = __builtin_amdgcn_make_buffer_rsrc(RST, 0, L * 8, 0x00020000);
  float n0[8], n1[8]; u32x2 st0, st1;
  ln_row(xr0, lane, n0, st0); ln_row(xr1, lane, n1, st1);
  if (lane == 0) __builtin_amdgcn_raw_buffer_store_b128((u32x4){st0.x, st0.y, st1.x, st1.y}, srs, (u32)((g * 8 + 2 * r) * 8), 0, 16);
  if (lane < 56) {
#pragma unroll
    for (int i = 0; i < 8; ++i) *(LAS u32*)(sm + LN_T + (8 * lane + (i ^ (lane & 7))) * 16 + r * 4) = pk_f16((f32x2){n0[i], n1[i]});
  }
  WAIT_LGKM0();
  lds_set(sm, K1_FL + 32 + r); lds_wait4(sm, K1_FL + 32);
  const __amdgpu_buffer_rsrc_t urs = __builtin_amdgcn_make_buffer_rsrc(UT, 0, 256 * UG, 0x00020000);
#pragma unroll
  for (int i = 0; i < 2; ++i) {
    const int e = (2 * r + i) * 64 + lane;
    if (e < 448) { u32x4 ov = *(LAS u32x4*)(sm + LN_T + (e ^ ((e >> 3) & 7)) * 16); __builtin_amdgcn_raw_buffer_store_b128(ov, urs, (u32)(g * UG + e * 16), 0, 16); }
  }
  WAIT_LGKM0();
  lds_set(sm, K1_FL + 36 + r);
}
DI void ln_publish(LAS char* sm, int lane, u32* fl, u32 tag) {
  u32 old = 0;
  if (lane == 0) old = __hip_atomic_fetch_add((LAS u32*)(sm + K1_FL + 52), 1u, __ATOMIC_RELAXED, __HIP_MEMORY_SCOPE_WORKGROUP);
  if (lane == 0 && old == 3) __hip_atomic_store((gu32*)fl, tag, RLX_AGENT);
}

#define PHD 3
DI void m_role(LAS char* sm, int wv, int lane, int pm, int pn, int pm2, int pc, int rot1, int rot2, u32& tag, const u16* x, const u16* w1, char* UT, char* RST, char* HF, const u16* w2, u32* flags) {
  const int r32 = lane & 31, h = lane >> 5;
  LAS char* xr = sm + G1_X + wv * 4096 + r32 * 128;
  const int xsw = (r32 >> 1) & 7;
  LAS char* xrs[4];
#pragma unroll
  for (int s = 0; s < 4; ++s) xrs[s] = xr + (((2 * s + h) ^ xsw) << 4);
  const int q = (lane & 15) >> 2, p = lane & 3, blk = (lane >> 4) & 1;
  LAS char* wbs[4];
#pragma unroll
  for (int c = 0; c < 4; ++c) wbs[c] = sm + G1_W + (8 * h + q) * 256 + ((((c + wv) & 3) ^ q) << 6) + blk * 32 + p * 8;
  struct Frag { s16x4 a[4][2]; };
  auto rd = [&](int s, Frag& f) {
#pragma unroll
    for (int c = 0; c < 4; ++c) {
      f.a[c][0] = __builtin_amdgcn_ds_read_tr16_b64_v4i16((LAS s16x4*)(wbs[c] + (s & 15) * 4096));
      f.a[c][1] = __builtin_amdgcn_ds_read_tr16_b64_v4i16((LAS s16x4*)(wbs[c] + (s & 15) * 4096 + 1024));
    }
  };
  f32x16 acc[4] = {};
  f32x16 csa = {};
  const s16x8 ones = {0x3f80, 0x3f80, 0x3f80, 0x3f80, 0x3f80, 0x3f80, 0x3f80, 0x3f80};
  Frag fA, fB;
  const u32 smb1 = (u32)(size_t)sm;
  u32 xoff[4];
#pragma unroll
  for (int i = 0; i < 4; ++i) { int r = 8 * i + (lane >> 3); xoff[i] = (u32)((pm * 128 + wv * 32 + r) * (D * 2) + (((lane & 7) ^ ((r >> 1) & 7)) << 4)); }
  const u32 w1off = (u32)((16 * wv + (lane >> 4)) * (FF * 2) + pn * 256 + (((lane & 15) ^ (((lane >> 4) & 3) << 2)) << 4));
  auto w1dma = [&](int st, int i) { int se = st + rot1; se = se >= 7 ? se - 7 : se; glds16((const char*)w1 + (se * 64 + 4 * i) * (FF * 2), w1off, smb1 + G1_W + (st & 3) * 16384 + wv * 4096 + i * 1024); };
  auto xdma = [&](int st, int i) { int se = st + rot1; se = se >= 7 ? se - 7 : se; glds16((const char*)x + se * 128, xoff[i], smb1 + G1_X + (st & 1) * 16384 + wv * 4096 + i * 1024); };
  __builtin_amdgcn_s_setprio(2);
#pragma unroll
  for (int i = 0; i < 4; ++i) xdma(0, i);
#pragma unroll
  for (int i = 0; i < 4; ++i) xdma(1, i);
  s16x8 xs[2][4];
  auto xrd = [&](int st) {
#pragma unroll
    for (int s4 = 0; s4 < 4; ++s4) xs[st & 1][s4] = *(LAS s16x8*)(xrs[s4] + (st & 1) * 16384);
  };
  WAIT_VM(4);
  xrd(0);
  WAIT_LGKM0();
#pragma unroll
  for (int i = 0; i < 4; ++i) xdma(2, i);
  u32 raw = epoch_load(flags);
  lds_wait4(sm, K1_FL + 128);
  rd(0, fA);
  u32 pre = 0;
  auto step = [&](int s, Frag& cur, Frag& nxt) {
    const int st = s >> 2, s4 = s & 3;
    if (s4 == 0 && st >= 1 && st <= 3) { lds_set(sm, K1_FL + 192 + 4 * (st - 1) + wv); pre = flag_pre(sm, K1_FL + 192 + 4 * (st - 1)); }
    if (s4 == 1 && st >= 1 && st <= 3) flag_chk(pre, sm, K1_FL + 192 + 4 * (st - 1));
    if (s4 == 2 && st < 6) {
      if (st == 0) WAIT_VM(4); else if (st == 1) WAIT_VM(6); else if (st <= 3) WAIT_VM(10); else if (st == 4) WAIT_VM(8); else { WAIT_VM(0); TAG_READY(raw, tag); }
      if (st >= 3) lds_set(sm, K1_FL + 128 + 4 * (st + 1) + wv);
      xrd(st + 1);
      pre = flag_pre(sm, K1_FL + 128 + 4 * (st + 1));
    }
    if (s4 == 3 && st < 6) flag_chk(pre, sm, K1_FL + 128 + 4 * (st + 1));
    if (s < 27) rd(s + 1, nxt);
    bf16x8 bx = __builtin_bit_cast(bf16x8, xs[st & 1][s4]);
#define MM(c) acc[c] = __builtin_amdgcn_mfma_f32_32x32x16_bf16(__builtin_bit_cast(bf16x8, __builtin_shufflevector(cur.a[c][0], cur.a[c][1], 0, 1, 2, 3, 4, 5, 6, 7)), bx, acc[c], 0, 0, 0)
    MM(0); MM(1); MM(2);
    __builtin_amdgcn_sched_group_barrier(0x8, 1, 0); __builtin_amdgcn_sched_group_barrier(0x100, 4, 0);
    __builtin_amdgcn_sched_group_barrier(0x8, 1, 0); __builtin_amdgcn_sched_group_barrier(0x100, 4, 0);
    __builtin_amdgcn_sched_group_barrier(0x8, 1, 0); __builtin_amdgcn_sched_group_barrier(0x100, 4, 0);
    __builtin_amdgcn_sched_barrier(0);
    if (s4 == 3 && st + 3 <= 6) { WAIT_LGKM0(); xdma(st + 3, 0); }
    if (s4 == 0 && st >= 1 && st + 2 <= 6) xdma(st + 2, 2);
    if ((s4 == 1 || s4 == 2) && st >= 1 && st <= 3) w1dma(st + 3, 2 * (s4 - 1));
    MM(3);
    __builtin_amdgcn_sched_barrier(0);
    if (s4 == 3 && st + 3 <= 6) xdma(st + 3, 1);
    if (s4 == 0 && st >= 1 && st + 2 <= 6) xdma(st + 2, 3);
    if ((s4 == 1 || s4 == 2) && st >= 1 && st <= 3) w1dma(st + 3, 2 * (s4 - 1) + 1);
#undef MM
    __builtin_amdgcn_sched_barrier(0);
  };
#pragma unroll
  for (int s = 0; s < 28; s += 2) {
    step(s, fA, fB);
    step(s + 1, fB, fA);
  }
  WAIT_LGKM0();
  lds_set(sm, K1_FL + 180 + wv);
  lds_wait4(sm, K1_FL + 176);
  {
    u32 nmb = *(LAS u32*)(sm + G1_RS + (32 * wv + r32) * 8);
    u32x2 cp[4];
#pragma unroll
    for (int c = 0; c < 4; ++c) cp[c] = *(LAS u32x2*)(sm + G1_CS + (32 * ((c + wv) & 3) + r32) * 8);
    nmb = h ? 0u : nmb;
    u32x4 bm = {nmb, nmb, 0u, 0u};
#pragma unroll
    for (int c = 0; c < 4; ++c) {
      u32x4 am = {cp[c].x, cp[c].y, 0u, 0u};
      acc[c] = __builtin_amdgcn_mfma_f32_32x32x16_bf16(__builtin_bit_cast(bf16x8, am), __builtin_bit_cast(bf16x8, bm), acc[c], 0, 0, 0);
    }
    const __amdgpu_buffer_rsrc_t hrs = __builtin_amdgcn_make_buffer_rsrc(HF, 0, L * FF * 2, 0x00020000);
    const u32 ho = (u32)(((pm * 4 + wv) * 112 + pn * 8) * 1024 + lane * 16);
#pragma unroll
    for (int c = 0; c < 4; ++c) {
      const int ct = (c + wv) & 3;
#pragma unroll
      for (int s2 = 0; s2 < 2; ++s2) {
        typedef __attribute__((ext_vector_type(2))) short s16x2;
        const s16x2 zz = {0, 0};
        u32x4 o;
#pragma unroll
        for (int i = 0; i < 4; ++i)
          o[i] = __builtin_bit_cast(u32, __builtin_elementwise_max(__builtin_bit_cast(s16x2, pk_bf16(acc[c][8 * s2 + 2 * i], acc[c][8 * s2 + 2 * i + 1])), zz));
        __builtin_amdgcn_raw_buffer_store_b128(o, hrs, ho + (2 * ct + s2) * 1024, 0, 16);
      }
    }
  }
#define MPRIO2 0
  __builtin_amdgcn_s_setprio(MPRIO2);
  const u32 smR = (u32)(size_t)sm + P2_R + wv * 24576;
  const u32 woff = (u32)((wv * 448 + (lane >> 3)) * (D * 2) + pc * 128 + (((lane & 7) ^ (((lane >> 4) & 1) << 2)) << 4));
  auto wdma = [&](int t) {
    int te = t + rot2; te = te >= 14 ? te - 14 : te;
#pragma unroll
    for (int i = 0; i < 4; ++i) glds16((const char*)w2 + (te * 32 + i * 8) * (D * 2), woff, smR + (t % 6) * 4096 + i * 1024);
  };
  lds_wait4(sm, K1_FL + 180);
  wdma(0); wdma(1);
  WAIT_VM(8);
  if (lane == 0) __hip_atomic_store((gu32*)(flags + FL_H + ((pm * 4 + wv) * 16 + pn) * FSP), tag, RLX_AGENT);
  wdma(2); wdma(3); wdma(4); wdma(5);
  poll_flags(flags + FL_H + ((2 * pm2 + (lane & 1)) * 16 + ((7 * wv) >> 1) + ((lane >> 1) & 3)) * FSP, tag);
  constexpr int PH = PHD, NB = PH + 1;
  const char* hb = HF + ((size_t)(pm2 * 2) * 112 + wv * 28) * 1024;
  const u32 lo = lane * 16;
  u32x4 hf[NB][4];
  auto hld = [&](int t, u32x4 (&d)[4]) {
    int te = t + rot2; te = te >= 14 ? te - 14 : te;
    const char* hp = hb + te * 2048;
    asm volatile("global_load_dwordx4 %0, %4, %5 sc1\n\tglobal_load_dwordx4 %1, %4, %6 sc1\n\tglobal_load_dwordx4 %2, %4, %5 offset:1024 sc1\n\tglobal_load_dwordx4 %3, %4, %6 offset:1024 sc1"
                 : "=&v"(d[0]), "=&v"(d[1]), "=&v"(d[2]), "=&v"(d[3]) : "v"(lo), "s"(hp), "s"(hp + 112 * 1024) : "memory");
  };
#pragma unroll
  for (int t = 0; t < PH; ++t) hld(t, hf[t]);
  LAS char* wb2[2];
#pragma unroll
  for (int c = 0; c < 2; ++c) wb2[c] = sm + P2_R + wv * 24576 + (4 * h + q) * 128 + ((c ^ ((q >> 1) & 1)) << 6) + blk * 32 + p * 8;
  f32x16 ac2[2][2] = {};
#pragma unroll
  for (int t = 0; t < 14; ++t) {
    int allow = 0;
    if (t < PH) { allow = 4 * (PH - 1 - t); for (int i = 0; i < t; ++i) allow += ((i + PH < 14) ? 4 : 0) + ((i >= 1 && i + 5 < 14) ? 4 : 0); }
    else { const int i0 = t - PH; allow = (i0 >= 1 && i0 + 5 < 14) ? 4 : 0; for (int i = i0 + 1; i < t; ++i) allow += ((i + PH < 14) ? 4 : 0) + ((i >= 1 && i + 5 < 14) ? 4 : 0); }
    u32x4 (&d)[4] = hf[t % NB];
    asm volatile("s_waitcnt vmcnt(%4)" : "+v"(d[0]), "+v"(d[1]), "+v"(d[2]), "+v"(d[3]) : "i"(allow) : "memory");
    s16x4 wf[2][2][2];
#pragma unroll
    for (int s2 = 0; s2 < 2; ++s2)
#pragma unroll
      for (int c = 0; c < 2; ++c) {
        wf[s2][c][0] = __builtin_amdgcn_ds_read_tr16_b64_v4i16((LAS s16x4*)(wb2[c] + (t % 6) * 4096 + s2 * 2048));
        wf[s2][c][1] = __builtin_amdgcn_ds_read_tr16_b64_v4i16((LAS s16x4*)(wb2[c] + (t % 6) * 4096 + s2 * 2048 + 1024));
      }
    __builtin_amdgcn_sched_barrier(0);
    if (t + PH < 14) hld(t + PH, hf[(t + PH) % NB]);
    __builtin_amdgcn_sched_barrier(0);
#pragma unroll
    for (int s2 = 0; s2 < 2; ++s2) {
#pragma unroll
      for (int c = 0; c < 2; ++c)
#pragma unroll
        for (int rt = 0; rt < 2; ++rt)
          ac2[c][rt] = __builtin_amdgcn_mfma_f32_32x32x16_bf16(__builtin_bit_cast(bf16x8, __builtin_shufflevector(wf[s2][c][0], wf[s2][c][1], 0, 1, 2, 3, 4, 5, 6, 7)),
                                                               __builtin_bit_cast(bf16x8, d[2 * s2 + rt]), ac2[c][rt], 0, 0, 0);
      __builtin_amdgcn_sched_barrier(0);
      if (s2 == 0 && t >= 1 && t + 5 < 14) wdma(t + 5);
      __builtin_amdgcn_sched_barrier(0);
    }
  }
#pragma unroll
  for (int rt = 0; rt < 2; ++rt) {
    const int row = 32 * rt + r32;
    LAS char* rg = sm + P2_R + wv * 24576 + row * 272 + 16 * h;
#pragma unroll
    for (int c = 0; c < 2; ++c)
#pragma unroll
      for (int g = 0; g < 4; ++g)
        *(LAS f32x4*)(rg + (32 * c + 8 * g) * 4) = (f32x4){ac2[c][rt][4 * g], ac2[c][rt][4 * g + 1], ac2[c][rt][4 * g + 2], ac2[c][rt][4 * g + 3]};
  }
}

DI float swap32_sum(float x) { auto r = __builtin_amdgcn_permlane32_swap(__float_as_uint(x), __float_as_uint(x), false, false); return __uint_as_float(r[0]) + __uint_as_float(r[1]); }
DI float swap16_sum(float x) { auto r = __builtin_amdgcn_permlane16_swap(__float_as_uint(x), __float_as_uint(x), false, false); return __uint_as_float(r[0]) + __uint_as_float(r[1]); }
struct SsmIn { u32 ldt, dsk, la[2], ai[2], cr[2], ci[2]; u32x2 c4r, c4i; u32x4 lut0; u32x2 lut1; };
DI void ssm_load(SsmIn& in, int kw, int lane, int d0, const u16* logA, const u16* Aim, const u16* Cr, const u16* Ci, const u16* logdt, const u16* Dskip) {
  const int ch = kw >> 2, wv = kw & 3, d = d0 + ch;
  in.ldt = logdt[d];
#pragma unroll
  for (int r = 0; r < 2; ++r) {
    const int n = d * NS + ((wv + 2 * r) & 3) * 64 + lane;
    in.la[r] = logA[n]; in.ai[r] = Aim[n]; in.cr[r] = Cr[n]; in.ci[r] = Ci[n];
  }
  in.c4r = *(const u32x2*)(Cr + d * NS + 4 * lane); in.c4i = *(const u32x2*)(Ci + d * NS + 4 * lane);
  in.dsk = Dskip[d];
  const u16* lp = &g_lut.v[ch][wv * 64 + lane][0];
  in.lut0 = *(const u32x4*)lp; in.lut1 = *(const u32x2*)(lp + 8);
}
#define DSR64(dst, base, off) asm volatile("ds_read_b64 %0, %1 offset:%2" : "=v"(dst) : "v"(base), "i"(off) : "memory")
#define DSR128(dst, base, off) asm volatile("ds_read_b128 %0, %1 offset:%2" : "=v"(dst) : "v"(base), "i"(off) : "memory")

DI void h_role(LAS char* sm, int kw, int lane, int bid, int pm, int pn, int pm2, int rot1, u32& tag, const u16* x, const u16* w1,
               const u16* logA, const u16* Aim, const u16* Cr, const u16* Ci, const u16* logdt, const u16* Dskip,
               char* UT, char* RST, u16* ST, u32* flags) {
  const int d0 = 2 * bid;
  const int wv = kw & 3, ch = kw >> 2, t = wv * 64 + lane;
  const int r32 = lane & 31, h = lane >> 5;
  const u32 smb = (u32)(size_t)sm;
  SsmIn in;
  u32 raw;
  if (kw >= 6) {
    __builtin_amdgcn_s_setprio(1);
    const u32 woff = (u32)((32 * (kw - 6) + (lane >> 4)) * (FF * 2) + pn * 256 + (((lane & 15) ^ (((lane >> 4) & 3) << 2)) << 4));
#pragma unroll
    for (int st = 0; st < 4; ++st) {
      int se = st + rot1; se = se >= 7 ? se - 7 : se;
      if (st == 3) lds_wait4(sm, K1_FL + 36);
#pragma unroll
      for (int i = 0; i < 8; ++i) glds16((const char*)w1 + (se * 64 + 4 * i) * (FF * 2), woff, smb + G1_W + st * 16384 + (kw - 6) * 8192 + i * 1024);
      if (st == 2) { WAIT_VM(16); *(volatile LAS u16*)(sm + K1_FL + 128 + 2 * (kw - 6)) = 0x0101;
                     WAIT_VM(8); *(volatile LAS u16*)(sm + K1_FL + 132 + 2 * (kw - 6)) = 0x0101; }
    }
    WAIT_VM(8); *(volatile LAS u16*)(sm + K1_FL + 136 + 2 * (kw - 6)) = 0x0101;
    WAIT_VM(0); *(volatile LAS u16*)(sm + K1_FL + 140 + 2 * (kw - 6)) = 0x0101;
    __builtin_amdgcn_s_setprio(0);
    raw = epoch_load(flags);
    ssm_load(in, kw, lane, d0, logA, Aim, Cr, Ci, logdt, Dskip);
  } else {
    __builtin_amdgcn_s_setprio(3);
    u32x4 xrow = *(const u32x4*)(x + (size_t)((pm * 16 + pn) * 8 + 2 * (kw & 3)) * D + 8 * min(lane, 55));
    u32x4 xrow1 = *(const u32x4*)(x + (size_t)((pm * 16 + pn) * 8 + 2 * (kw & 3) + 1) * D + 8 * min(lane, 55));
    ssm_load(in, kw, lane, d0, logA, Aim, Cr, Ci, logdt, Dskip);
    raw = epoch_load(flags);
    if (kw < 4) ln_group_rows(sm, xrow, xrow1, lane, kw, pm * 16 + pn, UT, RST);
    __builtin_amdgcn_s_setprio(0);
  }
  const float dt = rbf(exp_bf(__uint_as_float(in.ldt << 16)));
  float SG, F, invK;
  kgen_scales(dt, in.c4r, in.c4i, SG, F, invK);
  f32x16 tacc = {};
#define CPRIO 1
#define HPRIO 0
  __builtin_amdgcn_s_setprio(HPRIO);
  const int nrep = kw < 4 ? 1 : (kw < 6 ? 2 : 0);
#pragma unroll 1
  for (int rep = 0; rep < nrep; ++rep) {
    u32 o[53];
    const float zr = -rbf(exp_bf(__uint_as_float((rep ? in.la[1] : in.la[0]) << 16))) * dt, zi = __uint_as_float((rep ? in.ai[1] : in.ai[0]) << 16) * dt;
    cf E8 = cexp1(zr, zi, 8.f), e1 = cexp1(zr, -zi, 1.f), e256 = cexp1(zr, -zi, 256.f);
    cm m8 = mk(E8);
    cf E16 = cmul1(E8, m8); cm m16 = mk(E16);
    cf E24 = cmul1(E16, m8), E32 = cmul1(E16, m16); cm m32 = mk(E32);
    cm m64 = mk(cmul1(E32, m32));
    const u32 p8 = pkc(E8), r8 = pkr(E8), p16 = pkc(E16), r16 = pkr(E16), p24 = pkc(E24), r24 = pkr(E24);
    cf A0 = {16384.f, 0.f}, A1 = {E32.r * 16384.f, E32.i * 16384.f};
#pragma unroll
    for (int i = 0; i < 4; ++i) {
      const u32 a0 = pkc(A0), a1 = pkc(A1);
      o[8 * i + 0] = a0; o[8 * i + 1] = hmul(a0, p8, r8); o[8 * i + 2] = hmul(a0, p16, r16); o[8 * i + 3] = hmul(a0, p24, r24);
      o[8 * i + 4] = a1; o[8 * i + 5] = hmul(a1, p8, r8); o[8 * i + 6] = hmul(a1, p16, r16); o[8 * i + 7] = hmul(a1, p24, r24);
      if (i < 3) { A0 = cmul1(A0, m64); A1 = cmul1(A1, m64); }
    }
    if (rep == 0) {
      WAIT_VM(0); TAG_READY(raw, tag);
      if (kw < 4) ln_publish(sm, lane, flags + FL_U + (pm * 16 + pn) * FSP, tag);
    }
    cm m1 = mk(e1), m256 = mk(e256);
    cf e2 = cmul1(e1, m1); cm m2 = mk(e2);
    cf e3 = cmul1(e2, m1), e4 = cmul1(e2, m2);
    const u32 q1 = pkc(e1), s1 = pkr(e1), q2 = pkc(e2), s2 = pkr(e2), q3 = pkc(e3), s3 = pkr(e3);
    cf g0 = {__uint_as_float((rep ? in.cr[1] : in.cr[0]) << 16) * dt * SG, -(__uint_as_float((rep ? in.ci[1] : in.ci[0]) << 16) * dt) * SG};
    cf g4 = cmul1(g0, mk(e4));
    u32 b0 = pkc(g0), b4 = pkc(g4);
    o[32 + 0] = b0; o[32 + 1] = hmul(b0, q1, s1); o[32 + 2] = hmul(b0, q2, s2); o[32 + 3] = hmul(b0, q3, s3);
    o[32 + 4] = b4; o[32 + 5] = hmul(b4, q1, s1); o[32 + 6] = hmul(b4, q2, s2); o[32 + 7] = hmul(b4, q3, s3);
    g0 = cmul1(g0, m256); g4 = cmul1(g4, m256);
    b0 = pkc(g0); b4 = pkc(g4);
    o[32 + 8] = hmul(b0, q2, s2); o[32 + 9] = b4; o[32 + 10] = hmul(b4, q2, s2); o[32 + 11] = b0;
    g0 = cmul1(g0, m256); g4 = cmul1(g4, m256);
    o[32 + 12] = pkc(g0); o[32 + 19] = pkc(g4);
    g0 = cmul1(g0, m256); g4 = cmul1(g4, m256);
    o[32 + 13] = pkc(g0); o[32 + 20] = pkc(g4);
#pragma unroll
    for (int k = 4; k <= 8; ++k) { g0 = cmul1(g0, m256); o[42 + k] = pkc(g0); }
    asm volatile("" : "+v"(o[0]), "+v"(o[1]), "+v"(o[2]), "+v"(o[3]), "+v"(o[4]), "+v"(o[5]), "+v"(o[6]), "+v"(o[7]), "+v"(o[8]), "+v"(o[9]), "+v"(o[10]), "+v"(o[11]), "+v"(o[12]), "+v"(o[13]), "+v"(o[14]), "+v"(o[15]), "+v"(o[16]), "+v"(o[17]), "+v"(o[18]), "+v"(o[19]), "+v"(o[20]), "+v"(o[21]), "+v"(o[22]), "+v"(o[23]), "+v"(o[24]), "+v"(o[25]), "+v"(o[26]));
    asm volatile("" : "+v"(o[27]), "+v"(o[28]), "+v"(o[29]), "+v"(o[30]), "+v"(o[31]), "+v"(o[32]), "+v"(o[33]), "+v"(o[34]), "+v"(o[35]), "+v"(o[36]), "+v"(o[37]), "+v"(o[38]), "+v"(o[39]), "+v"(o[40]), "+v"(o[41]), "+v"(o[42]), "+v"(o[43]), "+v"(o[44]), "+v"(o[45]), "+v"(o[46]), "+v"(o[47]), "+v"(o[48]), "+v"(o[49]), "+v"(o[50]), "+v"(o[51]), "+v"(o[52]));
    const int job = kw + 2 * rep;
    const u32 reg = job >= 6 ? job - 4 : (job >= 4 ? job - 4 : (job >= 2 ? job - 2 : job + 2));
    const bool second = job == 2 || job == 3 || job >= 6;
    const u32 sf = second ? 236u : 216u + 4u * reg;
    if (second) lds_wait4(sm, K1_FL + 216 + 4 * reg);
    __builtin_amdgcn_s_setprio(3);
    LAS char* kg = sm + SSM0 + reg * KG_SZ;
    LAS u32* hw = (LAS u32*)(kg + lane * 4);
#pragma unroll
    for (int i = 0; i < 53; ++i) hw[i * (KG_ROW / 4)] = o[i];
    LAS char* pa = kg + (32 + min(r32, 20)) * KG_ROW + h * 16;
    LAS char* pb = kg + r32 * KG_ROW + h * 16;
    h16x8 fa[8], fb[8];
#pragma unroll
    for (int ks = 0; ks < 8; ++ks) { fa[ks] = *(LAS h16x8*)(pa + ks * 32); fb[ks] = *(LAS h16x8*)(pb + ks * 32); }
    WAIT_LGKM0();
    *(volatile LAS u32*)(sm + K1_FL + sf) = 0x01010101u;
#pragma unroll
    for (int ks = 0; ks < 8; ++ks) tacc = __builtin_amdgcn_mfma_f32_32x32x16_f16(fa[ks], fb[ks], tacc, 0, 0, 0);
    __builtin_amdgcn_s_setprio(HPRIO);
  }
  if (kw >= 6) { WAIT_VM(0); TAG_READY(raw, tag); }
  if (kw == 6) {
    __builtin_amdgcn_s_setprio(3);
    poll_flags(flags + FL_U + (pm * 16 + (lane & 15)) * FSP, tag);
    const __amdgpu_buffer_rsrc_t srs = __builtin_amdgcn_make_buffer_rsrc(RST, 0, L * 8, 0x00020000);
    u32x4 sv = __builtin_amdgcn_raw_buffer_load_b128(srs, (u32)((pm * 128 + 2 * lane) * 8), 0, 16);
    *(LAS u32x4*)(sm + G1_RS + lane * 16) = sv;
    poll_flags(flags + FL_C + min((128 * pn) / 56 + (lane & 3), 31) * FSP, tag);
    const __amdgpu_buffer_rsrc_t crs = __builtin_amdgcn_make_buffer_rsrc(RST + (WS_CS - WS_RINV), 0, FF * 8, 0x00020000);
    u32x4 cv = __builtin_amdgcn_raw_buffer_load_b128(crs, (u32)((pn * 128 + 2 * lane) * 8), 0, 16);
    *(LAS u32x4*)(sm + G1_CS + lane * 16) = cv;
    WAIT_LGKM0();
    *(volatile LAS u32*)(sm + K1_FL + 176) = 0x01010101u;
    __builtin_amdgcn_s_setprio(0);
  }
  if (kw >= 6) { __builtin_amdgcn_s_setprio(3); poll_flags(flags + FL_U + ((kw - 6) * 128 + lane) * FSP, tag); poll_flags(flags + FL_U + ((kw - 6) * 128 + 64 + lane) * FSP, tag); __builtin_amdgcn_s_setprio(0); }
  lds_bar8s(sm, K1_FL + 56, kw);
  __builtin_amdgcn_s_setprio(3);
#pragma unroll
  for (int i = 0; i < 12; ++i) {
    const int a = (i & 3) + 8 * (i >> 2) + 4 * h;
    if (a < 21 && kw < 6) *(LAS float*)(sm + K2_PT + kw * 2688 + (a * 32 + r32) * 4) = tacc[i];
  }
  lds_bar8(sm, K1_FL + 64, kw);
  u32x4 uv;
  {
    __builtin_amdgcn_s_setprio(3);
    const __amdgpu_buffer_rsrc_t urs = __builtin_amdgcn_make_buffer_rsrc(UT, 0, 256 * UG, 0x00020000);
    uv = __builtin_amdgcn_raw_buffer_load_b128(urs, (u32)((4 * lane + wv) * UG + (d0 + ch) * 16), 0, 16);
  }
  {
    LAS char* pb0 = sm + K2_PT + ch * 4 * 2688 + t * 16;
    f32x4 v = {0.f, 0.f, 0.f, 0.f};
    if (t < 168) { v = *(LAS f32x4*)(pb0) + *(LAS f32x4*)(pb0 + 2688); if (!ch) v += *(LAS f32x4*)(pb0 + 2 * 2688) + *(LAS f32x4*)(pb0 + 3 * 2688); v *= F; }
    if (t < 184) *(LAS f32x4*)(sm + K2_T + ch * 2944 + t * 16) = v;
  }
  lds_bar8(sm, K1_FL + 72, kw);
  {
    float v[11];
#pragma unroll
    for (int e = 0; e < 11; ++e) {
      u32 w = e < 8 ? in.lut0[e >> 1] : in.lut1[(e - 8) >> 1];
      u32 off = (e & 1) ? (w >> 16) : (w & 0xffffu);
      v[e] = *(LAS float*)(sm + K2_T + off);
    }
    u32 ev[5], od[5];
#pragma unroll
    for (int i = 0; i < 5; ++i) { ev[i] = pk_f16((f32x2){v[2 * i], v[2 * i + 1]}); od[i] = pk_f16((f32x2){v[2 * i + 1], v[2 * i + 2]}); }
    LAS char* kt = sm + CV_K + ch * KT_BYTES + t * 16;
    *(LAS u32x4*)(kt + 3 * KT_COPY) = (u32x4){ev[0], ev[1], ev[2], ev[3]};
    *(LAS u32x4*)(kt + 2 * KT_COPY) = (u32x4){od[0], od[1], od[2], od[3]};
    *(LAS u32x4*)(kt + 1 * KT_COPY) = (u32x4){ev[1], ev[2], ev[3], ev[4]};
    *(LAS u32x4*)(kt + 0 * KT_COPY) = (u32x4){od[1], od[2], od[3], od[4]};
    if (lane < 32) *(LAS u32x4*)(sm + CV_U + ch * 6144 + wv * 1536 + lane * 16) = (u32x4){0u, 0u, 0u, 0u};
    if (t < 16) *(LAS u32x4*)(sm + CV_K + ch * KT_BYTES + (t >> 2) * KT_COPY + 4096 + (t & 3) * 16) = (u32x4){0u, 0u, 0u, 0u};
  }
  *(LAS u32x4*)(sm + CV_U + ch * 6144 + wv * 1536 + 512 + lane * 16) = uv;
  lds_bar8(sm, K1_FL + 80, kw);
  __builtin_amdgcn_s_setprio(CPRIO);
  f32x16 accA = {}, accB = {};
  {
    const u32 ka = smb + CV_K + ch * KT_BYTES + (r32 & 3) * KT_COPY + (2044 + 8 * h - 4 * (r32 >> 2)) * 2 - 7 * 64;
    const u32 ub = smb + CV_U + ch * 6144 + h * 1536 + (32 + r32) * 16 - 7 * 16;
    u32 kb[3], uu[3];
#pragma unroll
    for (int i = 0; i < 3; ++i) { const int sg = 3 * wv + i, g = sg >= 4, dl0 = 8 * (g ? sg - 4 : sg); kb[i] = ka - dl0 * 64; uu[i] = ub + (32 * g - dl0) * 16; }
    u32x2 a0[2][4], a1[2][4]; u32x4 bb[2][4];
#define CV_RD(n, buf, m) do { DSR64(a0[buf][m], kb[(n) >> 2], (7 - 2 * ((n) & 3) - ((m) >> 1)) * 64 + ((m) & 1) * 32); \
                              DSR64(a1[buf][m], kb[(n) >> 2], (7 - 2 * ((n) & 3) - ((m) >> 1)) * 64 + ((m) & 1) * 32 + 8); \
                              DSR128(bb[buf][m], uu[(n) >> 2], (7 - 2 * ((n) & 3) - ((m) >> 1)) * 16 + ((m) & 1) * 3072); } while (0)
#define CV_WAIT(buf, cnt) asm volatile("s_waitcnt lgkmcnt(" #cnt ")" : "+v"(a0[buf][0]), "+v"(a0[buf][1]), "+v"(a0[buf][2]), "+v"(a0[buf][3]), "+v"(a1[buf][0]), "+v"(a1[buf][1]), "+v"(a1[buf][2]), "+v"(a1[buf][3]), \
                                       "+v"(bb[buf][0]), "+v"(bb[buf][1]), "+v"(bb[buf][2]), "+v"(bb[buf][3]) :: "memory")
#define CV_MM(acc, buf, m) acc = __builtin_amdgcn_mfma_f32_32x32x16_f16(__builtin_bit_cast(h16x8, (u32x4){a0[buf][m].x, a0[buf][m].y, a1[buf][m].x, a1[buf][m].y}), __builtin_bit_cast(h16x8, bb[buf][m]), acc, 0, 0, 0)
#define CV_BATCH(n, acc) do { CV_WAIT((n) & 1, 0); \
      CV_MM(acc, (n) & 1, 0); __builtin_amdgcn_sched_barrier(0); if ((n) < 11) { CV_RD((n) + 1, ((n) + 1) & 1, 0); CV_RD((n) + 1, ((n) + 1) & 1, 1); CV_RD((n) + 1, ((n) + 1) & 1, 2); CV_RD((n) + 1, ((n) + 1) & 1, 3); } __builtin_amdgcn_sched_barrier(0); \
      CV_MM(acc, (n) & 1, 1); CV_MM(acc, (n) & 1, 2); CV_MM(acc, (n) & 1, 3); __builtin_amdgcn_sched_barrier(0); } while (0)
    CV_RD(0, 0, 0); CV_RD(0, 0, 1); CV_RD(0, 0, 2); CV_RD(0, 0, 3);
    CV_BATCH(0, accA); CV_BATCH(1, accA); CV_BATCH(2, accA); CV_BATCH(3, accA);
    CV_BATCH(4, accB); CV_BATCH(5, accB); CV_BATCH(6, accB); CV_BATCH(7, accB);
    CV_BATCH(8, accB); CV_BATCH(9, accB); CV_BATCH(10, accB); CV_BATCH(11, accB);
  }
  lds_bar8(sm, K1_FL + 96, kw);
  {
    const int slot = wv == 0 ? 0 : wv + 1;
    LAS char* pp = sm + CV_P + ch * 20480 + slot * 4096 + r32 * 128;
    if (wv == 1) {
#pragma unroll
      for (int g = 0; g < 4; ++g) *(LAS f32x4*)(pp - 4096 + (((2 * g + h) ^ (r32 & 7)) << 4)) = (f32x4){accA[4 * g], accA[4 * g + 1], accA[4 * g + 2], accA[4 * g + 3]};
    } else {
      accB += accA;
    }
#pragma unroll
    for (int g = 0; g < 4; ++g) *(LAS f32x4*)(pp + (((2 * g + h) ^ (r32 & 7)) << 4)) = (f32x4){accB[4 * g], accB[4 * g + 1], accB[4 * g + 2], accB[4 * g + 3]};
  }
  lds_bar8(sm, K1_FL + 104, kw);
  {
    const int a = t >> 2, bq = t & 3, g = a >> 5, ar = a & 31;
    LAS char* pp = sm + CV_P + ch * 20480 + (g ? 2 * 4096 : 0) + ar * 128;
    const u32 o0 = ((2 * bq) ^ (ar & 7)) << 4, o1 = ((2 * bq + 1) ^ (ar & 7)) << 4;
    f32x4 y0 = *(LAS f32x4*)(pp + o0) + *(LAS f32x4*)(pp + 4096 + o0);
    f32x4 y1 = *(LAS f32x4*)(pp + o1) + *(LAS f32x4*)(pp + 4096 + o1);
    if (g) { y0 += *(LAS f32x4*)(pp + 8192 + o0); y1 += *(LAS f32x4*)(pp + 8192 + o1); }
    h16x8 u = *(LAS h16x8*)(sm + CV_U + ch * 6144 + bq * 1536 + (32 + a) * 16);
    const float ds = __uint_as_float(in.dsk << 16);
    float r[8];
#pragma unroll
    for (int i = 0; i < 8; ++i) { float y = (i < 4 ? y0[i & 3] : y1[i & 3]) * invK; r[i] = rbf(y) + rbf((float)u[i] * ds); }
    u32x4 ov = {pk_bf16(r[0], r[1]), pk_bf16(r[2], r[3]), pk_bf16(r[4], r[5]), pk_bf16(r[6], r[7])};
    const __amdgpu_buffer_rsrc_t rs = __builtin_amdgcn_make_buffer_rsrc(ST, 0, D * LP * 2, 0x00020000);
    __builtin_amdgcn_s_setprio(3);
    __builtin_amdgcn_raw_buffer_store_b128(ov, rs, (u32)(((d0 + ch) * LP + 8 * t) * 2), 0, 16);
  }
  WAIT_VM(0);
  {
    u32 old = 0;
    if (lane == 0) old = __hip_atomic_fetch_add((LAS u32*)(sm + K1_FL + 88), 1u, __ATOMIC_RELAXED, __HIP_MEMORY_SCOPE_WORKGROUP);
    if (lane == 0 && old == 7) __hip_atomic_store((gu32*)(flags + FL_S + bid * FSP), tag, RLX_AGENT);
  }
  __builtin_amdgcn_s_setprio(0);
}

#define ROT2 ((7 * (idx & 3) / 2 + 2 * (idx >> 2)) % 14)
__global__ __launch_bounds__(768, 3) void p_kernel(const u16* x, const u16* logA, const u16* Aim, const u16* Cr, const u16* Ci, const u16* logdt,
                                                   const u16* Dskip, const u16* w1, const u16* w2, char* ws, u16* out) {
  extern __shared__ __attribute__((aligned(16))) char smem[];
  LAS char* sm = (LAS char*)smem;
  const int tid = threadIdx.x, lane = tid & 63;
  const int wave = __builtin_amdgcn_readfirstlane(tid >> 6);
  const int bid = (int)blockIdx.x >= 32 ? (int)blockIdx.x - 32 : (int)blockIdx.x + 224;
  const int xcd = bid & 7, idx = bid >> 3;
  const int pm = (xcd >> 1) * 4 + (idx & 3), pn = (xcd & 1) * 7 + (idx >> 2);
  const int pm2 = xcd * 4 + (idx & 3), pc = idx >> 2;
  const int rot1 = (2 * (idx & 3) + (idx >> 2)) % 7, rot2 = ROT2;
  u32* flags = (u32*)(ws + WS_FLAG);
  u32 tag = 0;
  if (tid < 64) *(LAS u32*)(sm + K1_FL + tid * 4) = 0u;
  WAIT_LGKM0(); CBAR();
  if (bid >= 224) {
    if (wave < 4) {
      const int e = bid - 224, g = (e >> 1) * 16 + 14 + (e & 1);
      u32x4 xrow = *(const u32x4*)(x + (size_t)(g * 8 + 2 * wave) * D + 8 * min(lane, 55));
      u32x4 xrow1 = *(const u32x4*)(x + (size_t)(g * 8 + 2 * wave + 1) * D + 8 * min(lane, 55));
      tag = epoch_load(flags) + 1u;
      ln_group_rows(sm, xrow, xrow1, lane, wave, g, ws + WS_UT, ws + WS_RINV);
      WAIT_VM(0);
      ln_publish(sm, lane, flags + FL_U + g * FSP, tag);
    }
    {
      const int e = bid - 224;
      LAS float* ps = (LAS float*)(sm + 65536);
      if (wave >= 4) {
        const int tt = tid - 256, c = tt % 7, rg = tt / 7;
        if (rg < 64) {
          u32x4 v[7];
#pragma unroll
          for (int i = 0; i < 7; ++i) v[i] = *(const u32x4*)(w1 + (size_t)(rg * 7 + i) * FF + 56 * e + 8 * c);
          float a8[8] = {0.f, 0.f, 0.f, 0.f, 0.f, 0.f, 0.f, 0.f};
#pragma unroll
          for (int i = 0; i < 7; ++i)
#pragma unroll
            for (int j = 0; j < 4; ++j) { a8[2 * j] += __uint_as_float(v[i][j] << 16); a8[2 * j + 1] += __uint_as_float(v[i][j] & 0xffff0000u); }
          *(LAS f32x4*)(ps + rg * 56 + 8 * c) = (f32x4){a8[0], a8[1], a8[2], a8[3]};
          *(LAS f32x4*)(ps + rg * 56 + 8 * c + 4) = (f32x4){a8[4], a8[5], a8[6], a8[7]};
        }
        lds_bar8(sm, K1_FL + 64, wave - 4);
        if (wave == 4) {
          const u32 tg = epoch_load(flags) + 1u;
          float c0 = 0.f;
          const int col = min(lane, 55);
#pragma unroll 8
          for (int r = 0; r < 64; ++r) c0 += ps[r * 56 + col];
          float p0 = rbf(c0), r1 = c0 - p0, p1 = rbf(r1), p2 = r1 - p1;
          const __amdgpu_buffer_rsrc_t crs = __builtin_amdgcn_make_buffer_rsrc(ws + WS_CS, 0, FF * 8, 0x00020000);
          if (lane < 56) __builtin_amdgcn_raw_buffer_store_b64((u32x2){pk_bf16(p0, p1), pk_bf16(p2, 0.f)}, crs, (u32)((56 * e + lane) * 8), 0, 16);
          WAIT_VM(0);
          if (lane == 0) __hip_atomic_store((gu32*)(flags + FL_C + e * FSP), tg, RLX_AGENT);
        }
      }
    }
    return;
  }
  if (wave < 4) {
    m_role(sm, wave, lane, pm, pn, pm2, pc, rot1, rot2, tag, x, w1, ws + WS_UT, ws + WS_RINV, ws + WS_H, w2, flags);
  } else {
    h_role(sm, wave - 4, lane, xcd * 28 + idx, pm, pn, pm2, rot1, tag, x, w1, logA, Aim, Cr, Ci, logdt, Dskip, ws + WS_UT, ws + WS_RINV, (u16*)(ws + WS_ST), flags);
  }
  const int e = tid - 256, row = (e >> 3) & 63, cq = e & 7;
  u32x4 xv; float rinv = 0.f, sval[8];
  if (wave >= 4) {
    xv = *(const u32x4*)(x + (size_t)(pm2 * 64 + row) * D + pc * 64 + 8 * cq);
    const __amdgpu_buffer_rsrc_t srs = __builtin_amdgcn_make_buffer_rsrc(ws + WS_RINV, 0, L * 8, 0x00020000);
    rinv = __uint_as_float(__builtin_amdgcn_raw_buffer_load_b32(srs, (u32)((pm2 * 64 + row) * 8 + 4), 0, 16));
    if (wave == 4) poll_flags(flags + FL_S + (pc * 32 + (lane & 31)) * FSP, tag);
    lds_bar8s(sm, K1_FL + 120, wave - 4);
    const __amdgpu_buffer_rsrc_t rs = __builtin_amdgcn_make_buffer_rsrc(ws + WS_ST, 0, D * LP * 2, 0x00020000);
    u32x4 sv = __builtin_amdgcn_raw_buffer_load_b128(rs, (u32)(((pc * 64 + (e >> 3)) * LP + pm2 * 64 + 8 * (e & 7)) * 2), 0, 16);
    *(LAS u32x4*)(sm + K2_S + (e >> 3) * 144 + (e & 7) * 16) = sv;
    lds_bar8(sm, K1_FL + 112, wave - 4);
#pragma unroll
    for (int i = 0; i < 8; ++i) sval[i] = bf2f(*(LAS u16*)(sm + K2_S + (8 * cq + i) * 144 + row * 2));
  }
  WAIT_LGKM0(); CBAR();
  if (wave >= 4) {
    f32x4 z0 = {0.f, 0.f, 0.f, 0.f}, z1 = {0.f, 0.f, 0.f, 0.f};
    const u32 ro = P2_R + row * 272 + cq * 32, rstep = 24576;
#pragma unroll
    for (int w = 0; w < 4; ++w) {
      z0 += *(LAS f32x4*)(sm + ro + w * rstep);
      z1 += *(LAS f32x4*)(sm + ro + w * rstep + 16);
    }
    float r[8];
#pragma unroll
    for (int i = 0; i < 8; ++i) {
      float o = rbf((i < 4 ? z0[i & 3] : z1[i & 3]) * rinv);
      float g = rbf(o * __builtin_amdgcn_rcpf(1.f + __builtin_amdgcn_exp2f(-1.4426950408889634f * o)));
      float xx = (i & 1) ? __uint_as_float(xv[i >> 1] & 0xffff0000u) : __uint_as_float(xv[i >> 1] << 16);
      r[i] = xx + rbf(sval[i] * g);
    }
    *(u32x4*)(out + (size_t)(pm2 * 64 + row) * D + pc * 64 + 8 * cq) = (u32x4){pk_bf16(r[0], r[1]), pk_bf16(r[2], r[3]), pk_bf16(r[4], r[5]), pk_bf16(r[6], r[7])};
  } else if (bid == 0 && wave == 0) {
#pragma unroll 1
    for (int i = 0; i < 4; ++i) poll_flags(flags + FL_S + min(i * 64 + lane, 223) * FSP, tag);
    if (lane == 0) __hip_atomic_store((gu32*)flags, tag, RLX_AGENT);
  }
}
}

#define LDSZ(x) (x)
extern "C" void kernel_launch(void* const* d_in, const int* in_sizes, int n_in,
                              void* d_out, int out_size, void* d_ws, size_t ws_size,
                              hipStream_t stream) {
  (void)hipFuncSetAttribute((const void*)p_kernel, hipFuncAttributeMaxDynamicSharedMemorySize, LDSZ(P_LDS));
  p_kernel<<<256, 768, LDSZ(P_LDS), stream>>>((const u16*)d_in[0], (const u16*)d_in[1], (const u16*)d_in[2], (const u16*)d_in[3], (const u16*)d_in[4], (const u16*)d_in[5],
                                              (const u16*)d_in[6], (const u16*)d_in[8], (const u16*)d_in[9], (char*)d_ws, (u16*)d_out);
}
```

```cpp
#include <hip/hip_runtime.h>
#include <hip/hip_bf16.h>
#include <stdint.h>

namespace {
constexpr int L = 2048, D = 448, NS = 256, FF = 1792;
typedef unsigned short u16;
typedef unsigned int u32;
typedef __attribute__((ext_vector_type(2))) float f32x2;
typedef __attribute__((ext_vector_type(4))) float f32x4;
typedef __attribute__((ext_vector_type(16))) float f32x16;
typedef __attribute__((ext_vector_type(2))) _Float16 h16x2;
typedef __attribute__((ext_vector_type(8))) _Float16 h16x8;
typedef __attribute__((ext_vector_type(2))) __bf16 bf16x2;
typedef __attribute__((ext_vector_type(8))) __bf16 bf16x8;
typedef __attribute__((ext_vector_type(4))) short s16x4;
typedef __attribute__((ext_vector_type(8))) short s16x8;
typedef __attribute__((ext_vector_type(2))) u32 u32x2;
typedef __attribute__((ext_vector_type(4))) u32 u32x4;
#define LAS __attribute__((address_space(3)))
#define DI __device__ __forceinline__

constexpr size_t WS_UT = 0;
constexpr size_t WS_KT = 2u << 20;
constexpr size_t WS_KS = 10u << 20;
constexpr size_t WS_H = 4u << 20;
constexpr size_t WS_ST = 2u << 20;
constexpr size_t WS_FLAG = (2u << 20) - (128u << 10);
constexpr size_t WS_CS = (2u << 20) - (144u << 10);
constexpr size_t WS_RINV = (2u << 20) - (160u << 10);
constexpr size_t WS_W2F = 26u << 20;
constexpr int LP = L + 128;
constexpr int FSP = 16;
constexpr int KT_COPY = 4160;
constexpr int KT_BYTES = 4 * KT_COPY;

DI float bf2f(u16 v) { return __uint_as_float(((u32)v) << 16); }
DI float rbf(float f) { return (float)(__bf16)f; }
DI u32 pk_bf16(float a, float b) { f32x2 v = {a, b}; return __builtin_bit_cast(u32, __builtin_convertvector(v, bf16x2)); }
DI u32 pk_f16(f32x2 v) { return __builtin_bit_cast(u32, __builtin_convertvector(v, h16x2)); }

DI f32x2 cmul(f32x2 a, f32x2 b, f32x2 bs) {
  f32x2 t = a.xx * b;
  return __builtin_elementwise_fma(a.yy, bs, t);
}
DI f32x2 rot(f32x2 b) { f32x2 r = {-b.y, b.x}; return r; }

template <int CTRL> DI int dpp_i(int x) { return __builtin_amdgcn_update_dpp(0, x, CTRL, 0xf, 0xf, false); }
DI float wave_sum(float x) {
  x += __builtin_bit_cast(float, dpp_i<0xB1>(__builtin_bit_cast(int, x)));
  x += __builtin_bit_cast(float, dpp_i<0x4E>(__builtin_bit_cast(int, x)));
  x += __builtin_bit_cast(float, dpp_i<0x141>(__builtin_bit_cast(int, x)));
  x += __builtin_bit_cast(float, dpp_i<0x140>(__builtin_bit_cast(int, x)));
  int xi = __builtin_bit_cast(int, x);
  float a = __builtin_bit_cast(float, __builtin_amdgcn_readlane(xi, 0)), b = __builtin_bit_cast(float, __builtin_amdgcn_readlane(xi, 16));
  float c = __builtin_bit_cast(float, __builtin_amdgcn_readlane(xi, 32)), d = __builtin_bit_cast(float, __builtin_amdgcn_readlane(xi, 48));
  return (a + b) + (c + d);
}
DI int wave_max_i(int x) {
  x = max(x, dpp_i<0xB1>(x)); x = max(x, dpp_i<0x4E>(x)); x = max(x, dpp_i<0x141>(x)); x = max(x, dpp_i<0x140>(x));
  return max(max(__builtin_amdgcn_readlane(x, 0), __builtin_amdgcn_readlane(x, 16)), max(__builtin_amdgcn_readlane(x, 32), __builtin_amdgcn_readlane(x, 48)));
}

DI void glds16(const void* sbase, u32 voff, u32 ldst) {
  u32 keep;
  asm volatile("s_mov_b32 %0, m0\n\ts_mov_b32 m0, %3\n\ts_nop 0\n\tglobal_load_lds_dwordx4 %1, %2\n\ts_mov_b32 m0, %0"
               : "=&s"(keep) : "v"(voff), "s"(sbase), "s"(ldst) : "memory");
}
#define WAIT_VM(n) asm volatile("s_waitcnt vmcnt(" #n ")" ::: "memory")
#define WAIT_LGKM0() asm volatile("s_waitcnt lgkmcnt(0)" ::: "memory")

DI void lds_wait4(LAS char* sm, u32 off) {
  u32 addr = (u32)(size_t)sm + off, tv, td, ts;
  asm volatile("v_mov_b32 %0, %3\n"
               "WL%=:\n\ts_sleep 1\n\tds_read_b32 %1, %0\n\ts_waitcnt lgkmcnt(0)\n\tv_readfirstlane_b32 %2, %1\n\ts_cmp_lg_u32 %2, 0x01010101\n\ts_cbranch_scc1 WL%=\n"
               : "=&v"(tv), "=&v"(td), "=&s"(ts) : "s"(addr) : "memory", "scc");
}
DI u32 flag_pre(LAS char* sm, u32 off) { return *(volatile LAS u32*)(sm + off); }
DI void flag_chk(u32 v, LAS char* sm, u32 off) {
  u32 addr = (u32)(size_t)sm + off, tv, ts;
  asm volatile("v_readfirstlane_b32 %2, %0\n\ts_cmp_eq_u32 %2, 0x01010101\n\ts_cbranch_scc1 FD%=\n\tv_mov_b32 %1, %3\n"
               "FL%=:\n\tds_read_b32 %0, %1\n\ts_waitcnt lgkmcnt(0)\n\tv_readfirstlane_b32 %2, %0\n\ts_cmp_lg_u32 %2, 0x01010101\n\ts_cbranch_scc1 FL%=\nFD%=:\n"
               : "+v"(v), "=&v"(tv), "=&s"(ts) : "s"(addr) : "memory", "scc");
}
DI void lds_set(LAS char* sm, u32 off) {
  *(volatile LAS unsigned char*)(sm + off) = 1;
}
DI void lds_bar4(LAS char* sm, u32 off, u32 target, int lane) {
  WAIT_LGKM0();
  if (lane == 0) __hip_atomic_fetch_add((LAS u32*)(sm + off), 1u, __ATOMIC_RELAXED, __HIP_MEMORY_SCOPE_WORKGROUP);
  for (;;) {
    u32 v = *(volatile LAS u32*)(sm + off);
    if ((u32)__builtin_amdgcn_readfirstlane(v) >= target) break;
  }
  asm volatile("" ::: "memory");
}

constexpr u32 G1_W = 0;
constexpr u32 G1_X = 65536;
constexpr u32 KG_ROW = 272, KG_SZ = 53 * KG_ROW;
constexpr u32 LN_T = 49152;
constexpr u32 SSM0 = 98304;
constexpr u32 G1_CS = 159904;
constexpr u32 G1_RS = G1_CS + 1024;
constexpr u32 K1_FL = G1_RS + 1024;
constexpr u32 P_LDS = K1_FL + 256;
constexpr u32 CV_U = SSM0;
constexpr u32 CV_K = SSM0 + 12288;
constexpr u32 CV_P = CV_K;
constexpr u32 K2_PT = SSM0;
constexpr u32 K2_T = 151552;
constexpr u32 K2_S = SSM0;
constexpr u32 P2_R = 0;
constexpr int UG = 7424;
#define CBAR() do { asm volatile("" ::: "memory"); __builtin_amdgcn_s_barrier(); asm volatile("" ::: "memory"); } while (0)
typedef __attribute__((address_space(1))) u32 gu32;
#define RLX_AGENT __ATOMIC_RELAXED, __HIP_MEMORY_SCOPE_AGENT
constexpr int FL_H = 16 * FSP, FL_U = 1056 * FSP, FL_S = 1328 * FSP, FL_C = 1560 * FSP;
#define PSLEEP 8
DI u32 epoch_load(const u32* flags) { return __hip_atomic_load((const gu32*)flags, RLX_AGENT); }
#define TAG_READY(raw, tag) do { asm volatile("" : "+v"(raw)); tag = raw + 1u; } while (0)
DI void poll_flags(const u32* p, u32 tag) {
  for (u32 spins = 0; spins < (1u << 22); ++spins) {
    u32 v = __hip_atomic_load((const gu32*)p, RLX_AGENT);
    if (__all(v == tag)) break;
    __builtin_amdgcn_s_sleep(PSLEEP);
  }
  asm volatile("" ::: "memory");
}

struct LutT { u16 v[2][256][12]; };
constexpr LutT make_lut() {
  LutT t{};
  for (int ch = 0; ch < 2; ++ch)
    for (int th = 0; th < 256; ++th)
      for (int e = 0; e < 12; ++e) {
        int j = 2047 - 8 * th - e;
        int off = 22 * 128;
        if (j >= 0) {
          int p = j;
          if (j >= 256) { int sh = j >= 1024 ? 3 : (j >= 512 ? 2 : 1); int q = j >> sh, r = j & ((1 << sh) - 1), hf = 1 << (sh - 1); if (r > hf || (r == hf && (q & 1))) ++q; p = q << sh; }
          int b = (p & 255) >> 3, lo = p & 7, k = p >> 8, a = 0;
          if (k == 0) a = lo; else if (lo == 0) a = 10 + k; else if (k == 1) a = 7 + (lo >> 1); else a = 17 + k;
          off = (a * 32 + b) * 4;
        }
        t.v[ch][th][e] = (u16)(off + ch * 2944);
      }
  return t;
}
__device__ const LutT g_lut = make_lut();

DI f32x2 cexp_hw(float zr, float zi, float p) {
  float ph = zi * p;
  float k = __builtin_rintf(ph * 0.15915494309189535f);
  float r = __builtin_fmaf(ph, 0.15915494309189535f, -k);
  r = __builtin_fmaf(ph, 6.4206382433e-09f, r);
  float e = __builtin_amdgcn_exp2f(zr * p * 1.4426950408889634f);
  return (f32x2){e * __builtin_amdgcn_cosf(r), e * __builtin_amdgcn_sinf(r)};
}
DI void lds_bar8(LAS char* sm, u32 off, int kw) {
  WAIT_LGKM0();
  *(volatile LAS unsigned char*)(sm + off + kw) = 1;
  u32 addr = (u32)(size_t)sm + off, tv, td, te, ts;
  asm volatile("v_mov_b32 %0, %4\n"
               "WB%=:\n\ts_sleep 1\n\tds_read_b32 %1, %0\n\tds_read_b32 %2, %0 offset:4\n\ts_waitcnt lgkmcnt(0)\n\tv_and_b32 %1, %1, %2\n\ts_nop 0\n\tv_readfirstlane_b32 %3, %1\n\ts_cmp_lg_u32 %3, 0x01010101\n\ts_cbranch_scc1 WB%=\n"
               : "=&v"(tv), "=&v"(td), "=&v"(te), "=&s"(ts) : "s"(addr) : "memory", "scc");
}
DI void lds_bar8s(LAS char* sm, u32 off, int kw) {
  WAIT_LGKM0();
  *(volatile LAS unsigned char*)(sm + off + kw) = 1;
  u32 addr = (u32)(size_t)sm + off, tv, td, te, ts;
  asm volatile("v_mov_b32 %0, %4\n"
               "WS%=:\n\ts_sleep 2\n\tds_read_b32 %1, %0\n\tds_read_b32 %2, %0 offset:4\n\ts_waitcnt lgkmcnt(0)\n\tv_and_b32 %1, %1, %2\n\ts_nop 0\n\tv_readfirstlane_b32 %3, %1\n\ts_cmp_lg_u32 %3, 0x01010101\n\ts_cbranch_scc1 WS%=\n"
               : "=&v"(tv), "=&v"(td), "=&v"(te), "=&s"(ts) : "s"(addr) : "memory", "scc");
}
struct cf { float r, i; };
DI float amul(float a, float b) { float o; asm("v_mul_f32 %0, %1, %2" : "=v"(o) : "v"(a), "v"(b)); return o; }
DI void afmac(float& d, float a, float b) { asm("v_fmac_f32 %0, %1, %2" : "+v"(d) : "v"(a), "v"(b)); }
struct cm { float r, i, ni; };
DI cm mk(cf e) { return cm{e.r, e.i, -e.i}; }
DI cf cmul1(cf a, cm b) { cf o; o.r = amul(a.r, b.r); o.i = amul(a.r, b.i); afmac(o.r, a.i, b.ni); afmac(o.i, a.i, b.r); return o; }
DI u32 pkc(cf a) { return pk_f16((f32x2){a.r, a.i}); }
DI cf cexp1(float zr, float zi, float p) { f32x2 e = cexp_hw(zr, zi, p); return cf{e.x, e.y}; }

DI void kgen_scales(float dt, u32x2 c4r, u32x2 c4i, float& SG, float& F, float& invKS) {
  typedef __attribute__((ext_vector_type(2))) unsigned short u16x2;
  u16x2 m = __builtin_elementwise_max(__builtin_elementwise_max(__builtin_bit_cast(u16x2, c4r.x & 0x7fff7fffu), __builtin_bit_cast(u16x2, c4r.y & 0x7fff7fffu)),
                                      __builtin_elementwise_max(__builtin_bit_cast(u16x2, c4i.x & 0x7fff7fffu), __builtin_bit_cast(u16x2, c4i.y & 0x7fff7fffu)));
  float cmax = __uint_as_float((u32)wave_max_i((int)max((u32)m.x, (u32)m.y)) << 16) * dt;
  int ec = min(max((int)(__float_as_uint(cmax) >> 23), 14), 240);
  SG = __uint_as_float((u32)(267 - ec) << 23);
  F = __uint_as_float((u32)(127 - 23) << 23);
  invKS = __uint_as_float((u32)(ec - 3) << 23);
}
DI float exp_bf(float x) {
  float t = x * 1.4426950408889634f;
  float lo = __builtin_fmaf(x, 1.4426950408889634f, -t);
  lo = __builtin_fmaf(x, 1.925963033500e-08f, lo);
  float e = __builtin_amdgcn_exp2f(t);
  return __builtin_fmaf(e * lo, 0.6931471805599453f, e);
}
DI u32 hmul(u32 a, u32 m, u32 mr) {
  h16x2 av = __builtin_bit_cast(h16x2, a);
  h16x2 t = av.xx * __builtin_bit_cast(h16x2, m);
  return __builtin_bit_cast(u32, __builtin_elementwise_fma(av.yy, __builtin_bit_cast(h16x2, mr), t));
}
DI u32 pkr(cf a) { return pk_f16((f32x2){-a.i, a.r}); }

DI void ln_row(u32x4 xv, int lane, float (&nv)[8], u32x2& stat) {
  float v[8];
#pragma unroll
  for (int i = 0; i < 4; ++i) { v[2 * i] = __uint_as_float(xv[i] << 16); v[2 * i + 1] = __uint_as_float(xv[i] & 0xffff0000u); }
  float sm1 = ((v[0] + v[1]) + (v[2] + v[3])) + ((v[4] + v[5]) + (v[6] + v[7]));
  sm1 = lane < 56 ? sm1 : 0.f;
  const float mean = wave_sum(sm1) * (1.f / D);
  float sq = 0.f;
#pragma unroll
  for (int i = 0; i < 8; ++i) { float dd = v[i] - mean; sq = __builtin_fmaf(dd, dd, sq); }
  sq = lane < 56 ? sq : 0.f;
  const float var = wave_sum(sq) * (1.f / D);
  const float mb = rbf(mean), sd = rbf(__builtin_amdgcn_sqrtf(rbf(rbf(var) + 1e-5f))), rinv = __builtin_amdgcn_rcpf(sd);
#pragma unroll
  for (int i = 0; i < 8; ++i) { float dd = rbf(v[i] - mb); float qv = dd * rinv; float rr = __builtin_fmaf(-qv, sd, dd); nv[i] = rbf(__builtin_fmaf(rr, rinv, qv)); }
  stat = (u32x2){pk_bf16(-mb, -mb), __float_as_uint(rinv)};
}
DI void ln_group_rows(LAS char* sm, u32x4 xr0, u32x4 xr1, int lane, int r, int g, char* UT, char* RST) {
  const __amdgpu_buffer_rsrc_t srs = __builtin_amdgcn_make_buffer_rsrc(RST, 0, L * 8, 0x00020000);
  float n0[8], n1[8]; u32x2 st0, st1;
  ln_row(xr0, lane, n0, st0); ln_row(xr1, lane, n1, st1);
  if (lane == 0) __builtin_amdgcn_raw_buffer_store_b128((u32x4){st0.x, st0.y, st1.x, st1.y}, srs, (u32)((g * 8 + 2 * r) * 8), 0, 16);
  if (lane < 56) {
#pragma unroll
    for (int i = 0; i < 8; ++i) *(LAS u32*)(sm + LN_T + (8 * lane + (i ^ (lane & 7))) * 16 + r * 4) = pk_f16((f32x2){n0[i], n1[i]});
  }
  WAIT_LGKM0();
  lds_set(sm, K1_FL + 32 + r); lds_wait4(sm, K1_FL + 32);
  const __amdgpu_buffer_rsrc_t urs = __builtin_amdgcn_make_buffer_rsrc(UT, 0, 256 * UG, 0x00020000);
#pragma unroll
  for (int i = 0; i < 2; ++i) {
    const int e = (2 * r + i) * 64 + lane;
    if (e < 448) { u32x4 ov = *(LAS u32x4*)(sm + LN_T + (e ^ ((e >> 3) & 7)) * 16); __builtin_amdgcn_raw_buffer_store_b128(ov, urs, (u32)(g * UG + e * 16), 0, 16); }
  }
  WAIT_LGKM0();
  lds_set(sm, K1_FL + 36 + r);
}
DI void ln_publish(LAS char* sm, int lane, u32* fl, u32 tag) {
  u32 old = 0;
  if (lane == 0) old = __hip_atomic_fetch_add((LAS u32*)(sm + K1_FL + 52), 1u, __ATOMIC_RELAXED, __HIP_MEMORY_SCOPE_WORKGROUP);
  if (lane == 0 && old == 3) __hip_atomic_store((gu32*)fl, tag, RLX_AGENT);
}

#define PHD 3
DI void m_role(LAS char* sm, int wv, int lane, int pm, int pn, int pm2, int pc, int rot1, int rot2, u32& tag, const u16* x, const u16* w1, char* UT, char* RST, char* HF, const u16* w2, u32* flags) {
  const int r32 = lane & 31, h = lane >> 5;
  LAS char* xr = sm + G1_X + wv * 4096 + r32 * 128;
  const int xsw = (r32 >> 1) & 7;
  LAS char* xrs[4];
#pragma unroll
  for (int s = 0; s < 4; ++s) xrs[s] = xr + (((2 * s + h) ^ xsw) << 4);
  const int q = (lane & 15) >> 2, p = lane & 3, blk = (lane >> 4) & 1;
  LAS char* wbs[4];
#pragma unroll
  for (int c = 0; c < 4; ++c) wbs[c] = sm + G1_W + (8 * h + q) * 256 + ((((c + wv) & 3) ^ q) << 6) + blk * 32 + p * 8;
  struct Frag { s16x4 a[4][2]; };
  auto rd = [&](int s, Frag& f) {
#pragma unroll
    for (int c = 0; c < 4; ++c) {
      f.a[c][0] = __builtin_amdgcn_ds_read_tr16_b64_v4i16((LAS s16x4*)(wbs[c] + (s & 15) * 4096));
      f.a[c][1] = __builtin_amdgcn_ds_read_tr16_b64_v4i16((LAS s16x4*)(wbs[c] + (s & 15) * 4096 + 1024));
    }
  };
  f32x16 acc[4] = {};
  f32x16 csa = {};
  const s16x8 ones = {0x3f80, 0x3f80, 0x3f80, 0x3f80, 0x3f80, 0x3f80, 0x3f80, 0x3f80};
  Frag fA, fB;
  const u32 smb1 = (u32)(size_t)sm;
  u32 xoff[4];
#pragma unroll
  for (int i = 0; i < 4; ++i) { int r = 8 * i + (lane >> 3); xoff[i] = (u32)((pm * 128 + wv * 32 + r) * (D * 2) + (((lane & 7) ^ ((r >> 1) & 7)) << 4)); }
  const u32 w1off = (u32)((16 * wv + (lane >> 4)) * (FF * 2) + pn * 256 + (((lane & 15) ^ (((lane >> 4) & 3) << 2)) << 4));
  auto w1dma = [&](int st, int i) { int se = st + rot1; se = se >= 7 ? se - 7 : se; glds16((const char*)w1 + (se * 64 + 4 * i) * (FF * 2), w1off, smb1 + G1_W + (st & 3) * 16384 + wv * 4096 + i * 1024); };
  auto xdma = [&](int st, int i) { int se = st + rot1; se = se >= 7 ? se - 7 : se; glds16((const char*)x + se * 128, xoff[i], smb1 + G1_X + (st & 1) * 16384 + wv * 4096 + i * 1024); };
  __builtin_amdgcn_s_setprio(2);
#pragma unroll
  for (int i = 0; i < 4; ++i) xdma(0, i);
#pragma unroll
  for (int i = 0; i < 4; ++i) xdma(1, i);
  s16x8 xs[2][4];
  auto xrd = [&](int st) {
#pragma unroll
    for (int s4 = 0; s4 < 4; ++s4) xs[st & 1][s4] = *(LAS s16x8*)(xrs[s4] + (st & 1) * 16384);
  };
  WAIT_VM(4);
  xrd(0);
  WAIT_LGKM0();
#pragma unroll
  for (int i = 0; i < 4; ++i) xdma(2, i);
  lds_wait4(sm, K1_FL + 128);
  rd(0, fA);
  u32 pre = 0;
  auto step = [&](int s, Frag& cur, Frag& nxt) {
    const int st = s >> 2, s4 = s & 3;
    if (s4 == 0 && st >= 1 && st <= 3) { lds_set(sm, K1_FL + 192 + 4 * (st - 1) + wv); pre = flag_pre(sm, K1_FL + 192 + 4 * (st - 1)); }
    if (s4 == 1 && st >= 1 && st <= 3) flag_chk(pre, sm, K1_FL + 192 + 4 * (st - 1));
    if (s4 == 2 && st < 6) {
      if (st == 0) WAIT_VM(4); else if (st == 1) WAIT_VM(6); else if (st <= 3) WAIT_VM(10); else if (st == 4) WAIT_VM(8); else WAIT_VM(0);
      if (st >= 3) lds_set(sm, K1_FL + 128 + 4 * (st + 1) + wv);
      xrd(st + 1);
      pre = flag_pre(sm, K1_FL + 128 + 4 * (st + 1));
    }
    if (s4 == 3 && st < 6) flag_chk(pre, sm, K1_FL + 128 + 4 * (st + 1));
    if (s < 27) rd(s + 1, nxt);
    bf16x8 bx = __builtin_bit_cast(bf16x8, xs[st & 1][s4]);
#define MM(c) acc[c] = __builtin_amdgcn_mfma_f32_32x32x16_bf16(__builtin_bit_cast(bf16x8, __builtin_shufflevector(cur.a[c][0], cur.a[c][1], 0, 1, 2, 3, 4, 5, 6, 7)), bx, acc[c], 0, 0, 0)
    MM(0); MM(1); MM(2);
    __builtin_amdgcn_sched_group_barrier(0x8, 1, 0); __builtin_amdgcn_sched_group_barrier(0x100, 4, 0);
    __builtin_amdgcn_sched_group_barrier(0x8, 1, 0); __builtin_amdgcn_sched_group_barrier(0x100, 4, 0);
    __builtin_amdgcn_sched_group_barrier(0x8, 1, 0); __builtin_amdgcn_sched_group_barrier(0x100, 4, 0);
    __builtin_amdgcn_sched_barrier(0);
    if (s4 == 3 && st + 3 <= 6) { WAIT_LGKM0(); xdma(st + 3, 0); }
    if (s4 == 0 && st >= 1 && st + 2 <= 6) xdma(st + 2, 2);
    if ((s4 == 1 || s4 == 2) && st >= 1 && st <= 3) w1dma(st + 3, 2 * (s4 - 1));
    MM(3);
    __builtin_amdgcn_sched_barrier(0);
    if (s4 == 3 && st + 3 <= 6) xdma(st + 3, 1);
    if (s4 == 0 && st >= 1 && st + 2 <= 6) xdma(st + 2, 3);
    if ((s4 == 1 || s4 == 2) && st >= 1 && st <= 3) w1dma(st + 3, 2 * (s4 - 1) + 1);
#undef MM
    __builtin_amdgcn_sched_barrier(0);
  };
#pragma unroll
  for (int s = 0; s < 28; s += 2) {
    step(s, fA, fB);
    step(s + 1, fB, fA);
  }
  WAIT_LGKM0();
  lds_set(sm, K1_FL + 180 + wv);
  lds_wait4(sm, K1_FL + 176);
  tag = *(volatile LAS u32*)(sm + K1_FL + 184);
  {
    u32 nmb = *(LAS u32*)(sm + G1_RS + (32 * wv + r32) * 8);
    u32x2 cp[4];
#pragma unroll
    for (int c = 0; c < 4; ++c) cp[c] = *(LAS u32x2*)(sm + G1_CS + (32 * ((c + wv) & 3) + r32) * 8);
    nmb = h ? 0u : nmb;
    u32x4 bm = {nmb, nmb, 0u, 0u};
#pragma unroll
    for (int c = 0; c < 4; ++c) {
      u32x4 am = {cp[c].x, cp[c].y, 0u, 0u};
      acc[c] = __builtin_amdgcn_mfma_f32_32x32x16_bf16(__builtin_bit_cast(bf16x8, am), __builtin_bit_cast(bf16x8, bm), acc[c], 0, 0, 0);
    }
    const __amdgpu_buffer_rsrc_t hrs = __builtin_amdgcn_make_buffer_rsrc(HF, 0, L * FF * 2, 0x00020000);
    const u32 ho = (u32)(((pm * 4 + wv) * 112 + pn * 8) * 1024 + lane * 16);
#pragma unroll
    for (int c = 0; c < 4; ++c) {
      const int ct = (c + wv) & 3;
#pragma unroll
      for (int s2 = 0; s2 < 2; ++s2) {
        typedef __attribute__((ext_vector_type(2))) short s16x2;
        const s16x2 zz = {0, 0};
        u32x4 o;
#pragma unroll
        for (int i = 0; i < 4; ++i)
          o[i] = __builtin_bit_cast(u32, __builtin_elementwise_max(__builtin_bit_cast(s16x2, pk_bf16(acc[c][8 * s2 + 2 * i], acc[c][8 * s2 + 2 * i + 1])), zz));
        __builtin_amdgcn_raw_buffer_store_b128(o, hrs, ho + (2 * ct + s2) * 1024, 0, 16);
      }
    }
  }
#define MPRIO2 0
  __builtin_amdgcn_s_setprio(MPRIO2);
  const u32 smR = (u32)(size_t)sm + P2_R + wv * 24576;
  const u32 woff = (u32)((wv * 448 + (lane >> 3)) * (D * 2) + pc * 128 + (((lane & 7) ^ (((lane >> 4) & 1) << 2)) << 4));
  auto wdma = [&](int t) {
    int te = t + rot2; te = te >= 14 ? te - 14 : te;
#pragma unroll
    for (int i = 0; i < 4; ++i) glds16((const char*)w2 + (te * 32 + i * 8) * (D * 2), woff, smR + (t % 6) * 4096 + i * 1024);
  };
  lds_wait4(sm, K1_FL + 180);
  wdma(0); wdma(1);
  WAIT_VM(8);
  if (lane == 0) __hip_atomic_store((gu32*)(flags + FL_H + ((pm * 4 + wv) * 16 + pn) * FSP), tag, RLX_AGENT);
  wdma(2); wdma(3); wdma(4); wdma(5);
  poll_flags(flags + FL_H + ((2 * pm2 + (lane & 1)) * 16 + ((7 * wv) >> 1) + ((lane >> 1) & 3)) * FSP, tag);
  constexpr int PH = PHD, NB = PH + 1;
  const char* hb = HF + ((size_t)(pm2 * 2) * 112 + wv * 28) * 1024;
  const u32 lo = lane * 16;
  u32x4 hf[NB][4];
  auto hld = [&](int t, u32x4 (&d)[4]) {
    int te = t + rot2; te = te >= 14 ? te - 14 : te;
    const char* hp = hb + te * 2048;
    asm volatile("global_load_dwordx4 %0, %4, %5 sc1\n\tglobal_load_dwordx4 %1, %4, %6 sc1\n\tglobal_load_dwordx4 %2, %4, %5 offset:1024 sc1\n\tglobal_load_dwordx4 %3, %4, %6 offset:1024 sc1"
                 : "=&v"(d[0]), "=&v"(d[1]), "=&v"(d[2]), "=&v"(d[3]) : "v"(lo), "s"(hp), "s"(hp + 112 * 1024) : "memory");
  };
#pragma unroll
  for (int t = 0; t < PH; ++t) hld(t, hf[t]);
  LAS char* wb2[2];
#pragma unroll
  for (int c = 0; c < 2; ++c) wb2[c] = sm + P2_R + wv * 24576 + (4 * h + q) * 128 + ((c ^ ((q >> 1) & 1)) << 6) + blk * 32 + p * 8;
  f32x16 ac2[2][2] = {};
#pragma unroll
  for (int t = 0; t < 14; ++t) {
    int allow = 0;
    if (t < PH) { allow = 4 * (PH - 1 - t); for (int i = 0; i < t; ++i) allow += ((i + PH < 14) ? 4 : 0) + ((i >= 1 && i + 5 < 14) ? 4 : 0); }
    else { const int i0 = t - PH; allow = (i0 >= 1 && i0 + 5 < 14) ? 4 : 0; for (int i = i0 + 1; i < t; ++i) allow += ((i + PH < 14) ? 4 : 0) + ((i >= 1 && i + 5 < 14) ? 4 : 0); }
    u32x4 (&d)[4] = hf[t % NB];
    asm volatile("s_waitcnt vmcnt(%4)" : "+v"(d[0]), "+v"(d[1]), "+v"(d[2]), "+v"(d[3]) : "i"(allow) : "memory");
    s16x4 wf[2][2][2];
#pragma unroll
    for (int s2 = 0; s2 < 2; ++s2)
#pragma unroll
      for (int c = 0; c < 2; ++c) {
        wf[s2][c][0] = __builtin_amdgcn_ds_read_tr16_b64_v4i16((LAS s16x4*)(wb2[c] + (t % 6) * 4096 + s2 * 2048));
        wf[s2][c][1] = __builtin_amdgcn_ds_read_tr16_b64_v4i16((LAS s16x4*)(wb2[c] + (t % 6) * 4096 + s2 * 2048 + 1024));
      }
    __builtin_amdgcn_sched_barrier(0);
    if (t + PH < 14) hld(t + PH, hf[(t + PH) % NB]);
    __builtin_amdgcn_sched_barrier(0);
#pragma unroll
    for (int s2 = 0; s2 < 2; ++s2) {
#pragma unroll
      for (int c = 0; c < 2; ++c)
#pragma unroll
        for (int rt = 0; rt < 2; ++rt)
          ac2[c][rt] = __builtin_amdgcn_mfma_f32_32x32x16_bf16(__builtin_bit_cast(bf16x8, __builtin_shufflevector(wf[s2][c][0], wf[s2][c][1], 0, 1, 2, 3, 4, 5, 6, 7)),
                                                               __builtin_bit_cast(bf16x8, d[2 * s2 + rt]), ac2[c][rt], 0, 0, 0);
      __builtin_amdgcn_sched_barrier(0);
      if (s2 == 0 && t >= 1 && t + 5 < 14) wdma(t + 5);
      __builtin_amdgcn_sched_barrier(0);
    }
  }
#pragma unroll
  for (int rt = 0; rt < 2; ++rt) {
    const int row = 32 * rt + r32;
    LAS char* rg = sm + P2_R + wv * 24576 + row * 272 + 16 * h;
#pragma unroll
    for (int c = 0; c < 2; ++c)
#pragma unroll
      for (int g = 0; g < 4; ++g)
        *(LAS f32x4*)(rg + (32 * c + 8 * g) * 4) = (f32x4){ac2[c][rt][4 * g], ac2[c][rt][4 * g + 1], ac2[c][rt][4 * g + 2], ac2[c][rt][4 * g + 3]};
  }
}

DI float swap32_sum(float x) { auto r = __builtin_amdgcn_permlane32_swap(__float_as_uint(x), __float_as_uint(x), false, false); return __uint_as_float(r[0]) + __uint_as_float(r[1]); }
DI float swap16_sum(float x) { auto r = __builtin_amdgcn_permlane16_swap(__float_as_uint(x), __float_as_uint(x), false, false); return __uint_as_float(r[0]) + __uint_as_float(r[1]); }
struct SsmIn { u32 ldt, dsk, la[2], ai[2], cr[2], ci[2]; u32x2 c4r, c4i; u32x4 lut0; u32x2 lut1; };
DI void ssm_load(SsmIn& in, int kw, int lane, int d0, const u16* logA, const u16* Aim, const u16* Cr, const u16* Ci, const u16* logdt, const u16* Dskip) {
  const int ch = kw >> 2, wv = kw & 3, d = d0 + ch;
  in.ldt = logdt[d];
#pragma unroll
  for (int r = 0; r < 2; ++r) {
    const int n = d * NS + ((wv + 2 * r) & 3) * 64 + lane;
    in.la[r] = logA[n]; in.ai[r] = Aim[n]; in.cr[r] = Cr[n]; in.ci[r] = Ci[n];
  }
  in.c4r = *(const u32x2*)(Cr + d * NS + 4 * lane); in.c4i = *(const u32x2*)(Ci + d * NS + 4 * lane);
  in.dsk = Dskip[d];
  const u16* lp = &g_lut.v[ch][wv * 64 + lane][0];
  in.lut0 = *(const u32x4*)lp; in.lut1 = *(const u32x2*)(lp + 8);
}
#define DSR64(dst, base, off) asm volatile("ds_read_b64 %0, %1 offset:%2" : "=v"(dst) : "v"(base), "i"(off) : "memory")
#define DSR128(dst, base, off) asm volatile("ds_read_b128 %0, %1 offset:%2" : "=v"(dst) : "v"(base), "i"(off) : "memory")

DI void h_role(LAS char* sm, int kw, int lane, int bid, int pm, int pn, int pm2, int rot1, u32& tag, const u16* x, const u16* w1,
               const u16* logA, const u16* Aim, const u16* Cr, const u16* Ci, const u16* logdt, const u16* Dskip,
               char* UT, char* RST, u16* ST, u32* flags) {
  const int d0 = 2 * bid;
  const int wv = kw & 3, ch = kw >> 2, t = wv * 64 + lane;
  const int r32 = lane & 31, h = lane >> 5;
  const u32 smb = (u32)(size_t)sm;
  SsmIn in;
  u32 raw;
  if (kw >= 6) {
    __builtin_amdgcn_s_setprio(1);
    const u32 woff = (u32)((32 * (kw - 6) + (lane >> 4)) * (FF * 2) + pn * 256 + (((lane & 15) ^ (((lane >> 4) & 3) << 2)) << 4));
#pragma unroll
    for (int st = 0; st < 4; ++st) {
      int se = st + rot1; se = se >= 7 ? se - 7 : se;
      if (st == 3) lds_wait4(sm, K1_FL + 36);
#pragma unroll
      for (int i = 0; i < 8; ++i) glds16((const char*)w1 + (se * 64 + 4 * i) * (FF * 2), woff, smb + G1_W + st * 16384 + (kw - 6) * 8192 + i * 1024);
      if (st == 2) { WAIT_VM(16); *(volatile LAS u16*)(sm + K1_FL + 128 + 2 * (kw - 6)) = 0x0101;
                     WAIT_VM(8); *(volatile LAS u16*)(sm + K1_FL + 132 + 2 * (kw - 6)) = 0x0101; }
    }
    WAIT_VM(8); *(volatile LAS u16*)(sm + K1_FL + 136 + 2 * (kw - 6)) = 0x0101;
    WAIT_VM(0); *(volatile LAS u16*)(sm + K1_FL + 140 + 2 * (kw - 6)) = 0x0101;
    __builtin_amdgcn_s_setprio(0);
    raw = epoch_load(flags);
    ssm_load(in, kw, lane, d0, logA, Aim, Cr, Ci, logdt, Dskip);
  } else {
    __builtin_amdgcn_s_setprio(3);
    u32x4 xrow = *(const u32x4*)(x + (size_t)((pm * 16 + pn) * 8 + 2 * (kw & 3)) * D + 8 * min(lane, 55));
    u32x4 xrow1 = *(const u32x4*)(x + (size_t)((pm * 16 + pn) * 8 + 2 * (kw & 3) + 1) * D + 8 * min(lane, 55));
    ssm_load(in, kw, lane, d0, logA, Aim, Cr, Ci, logdt, Dskip);
    raw = epoch_load(flags);
    if (kw < 4) ln_group_rows(sm, xrow, xrow1, lane, kw, pm * 16 + pn, UT, RST);
    __builtin_amdgcn_s_setprio(0);
  }
  const float dt = rbf(exp_bf(__uint_as_float(in.ldt << 16)));
  float SG, F, invK;
  kgen_scales(dt, in.c4r, in.c4i, SG, F, invK);
  f32x16 tacc = {};
#define CPRIO 1
#define HPRIO 0
  __builtin_amdgcn_s_setprio(HPRIO);
  const int nrep = kw < 4 ? 1 : (kw < 6 ? 2 : 0);
#pragma unroll 1
  for (int rep = 0; rep < nrep; ++rep) {
    u32 o[53];
    const float zr = -rbf(exp_bf(__uint_as_float((rep ? in.la[1] : in.la[0]) << 16))) * dt, zi = __uint_as_float((rep ? in.ai[1] : in.ai[0]) << 16) * dt;
    cf E8 = cexp1(zr, zi, 8.f), e1 = cexp1(zr, -zi, 1.f), e256 = cexp1(zr, -zi, 256.f);
    cm m8 = mk(E8);
    cf E16 = cmul1(E8, m8); cm m16 = mk(E16);
    cf E24 = cmul1(E16, m8), E32 = cmul1(E16, m16); cm m32 = mk(E32);
    cm m64 = mk(cmul1(E32, m32));
    const u32 p8 = pkc(E8), r8 = pkr(E8), p16 = pkc(E16), r16 = pkr(E16), p24 = pkc(E24), r24 = pkr(E24);
    cf A0 = {16384.f, 0.f}, A1 = {E32.r * 16384.f, E32.i * 16384.f};
#pragma unroll
    for (int i = 0; i < 4; ++i) {
      const u32 a0 = pkc(A0), a1 = pkc(A1);
      o[8 * i + 0] = a0; o[8 * i + 1] = hmul(a0, p8, r8); o[8 * i + 2] = hmul(a0, p16, r16); o[8 * i + 3] = hmul(a0, p24, r24);
      o[8 * i + 4] = a1; o[8 * i + 5] = hmul(a1, p8, r8); o[8 * i + 6] = hmul(a1, p16, r16); o[8 * i + 7] = hmul(a1, p24, r24);
      if (i < 3) { A0 = cmul1(A0, m64); A1 = cmul1(A1, m64); }
    }
    if (rep == 0) {
      WAIT_VM(0); TAG_READY(raw, tag);
      if (kw < 4) ln_publish(sm, lane, flags + FL_U + (pm * 16 + pn) * FSP, tag);
    }
    cm m1 = mk(e1), m256 = mk(e256);
    cf e2 = cmul1(e1, m1); cm m2 = mk(e2);
    cf e3 = cmul1(e2, m1), e4 = cmul1(e2, m2);
    const u32 q1 = pkc(e1), s1 = pkr(e1), q2 = pkc(e2), s2 = pkr(e2), q3 = pkc(e3), s3 = pkr(e3);
    cf g0 = {__uint_as_float((rep ? in.cr[1] : in.cr[0]) << 16) * dt * SG, -(__uint_as_float((rep ? in.ci[1] : in.ci[0]) << 16) * dt) * SG};
    cf g4 = cmul1(g0, mk(e4));
    u32 b0 = pkc(g0), b4 = pkc(g4);
    o[32 + 0] = b0; o[32 + 1] = hmul(b0, q1, s1); o[32 + 2] = hmul(b0, q2, s2); o[32 + 3] = hmul(b0, q3, s3);
    o[32 + 4] = b4; o[32 + 5] = hmul(b4, q1, s1); o[32 + 6] = hmul(b4, q2, s2); o[32 + 7] = hmul(b4, q3, s3);
    g0 = cmul1(g0, m256); g4 = cmul1(g4, m256);
    b0 = pkc(g0); b4 = pkc(g4);
    o[32 + 8] = hmul(b0, q2, s2); o[32 + 9] = b4; o[32 + 10] = hmul(b4, q2, s2); o[32 + 11] = b0;
    g0 = cmul1(g0, m256); g4 = cmul1(g4, m256);
    o[32 + 12] = pkc(g0); o[32 + 19] = pkc(g4);
    g0 = cmul1(g0, m256); g4 = cmul1(g4, m256);
    o[32 + 13] = pkc(g0); o[32 + 20] = pkc(g4);
#pragma unroll
    for (int k = 4; k <= 8; ++k) { g0 = cmul1(g0, m256); o[42 + k] = pkc(g0); }
    asm volatile("" : "+v"(o[0]), "+v"(o[1]), "+v"(o[2]), "+v"(o[3]), "+v"(o[4]), "+v"(o[5]), "+v"(o[6]), "+v"(o[7]), "+v"(o[8]), "+v"(o[9]), "+v"(o[10]), "+v"(o[11]), "+v"(o[12]), "+v"(o[13]), "+v"(o[14]), "+v"(o[15]), "+v"(o[16]), "+v"(o[17]), "+v"(o[18]), "+v"(o[19]), "+v"(o[20]), "+v"(o[21]), "+v"(o[22]), "+v"(o[23]), "+v"(o[24]), "+v"(o[25]), "+v"(o[26]));
    asm volatile("" : "+v"(o[27]), "+v"(o[28]), "+v"(o[29]), "+v"(o[30]), "+v"(o[31]), "+v"(o[32]), "+v"(o[33]), "+v"(o[34]), "+v"(o[35]), "+v"(o[36]), "+v"(o[37]), "+v"(o[38]), "+v"(o[39]), "+v"(o[40]), "+v"(o[41]), "+v"(o[42]), "+v"(o[43]), "+v"(o[44]), "+v"(o[45]), "+v"(o[46]), "+v"(o[47]), "+v"(o[48]), "+v"(o[49]), "+v"(o[50]), "+v"(o[51]), "+v"(o[52]));
    const int job = kw + 2 * rep;
    const u32 reg = job >= 6 ? job - 4 : (job >= 4 ? job - 4 : (job >= 2 ? job - 2 : job + 2));
    const bool second = job == 2 || job == 3 || job >= 6;
    const u32 sf = second ? 236u : 216u + 4u * reg;
    if (second) lds_wait4(sm, K1_FL + 216 + 4 * reg);
    __builtin_amdgcn_s_setprio(3);
    LAS char* kg = sm + SSM0 + reg * KG_SZ;
    LAS u32* hw = (LAS u32*)(kg + lane * 4);
#pragma unroll
    for (int i = 0; i < 53; ++i) hw[i * (KG_ROW / 4)] = o[i];
    LAS char* pa = kg + (32 + min(r32, 20)) * KG_ROW + h * 16;
    LAS char* pb = kg + r32 * KG_ROW + h * 16;
    h16x8 fa[8], fb[8];
#pragma unroll
    for (int ks = 0; ks < 8; ++ks) { fa[ks] = *(LAS h16x8*)(pa + ks * 32); fb[ks] = *(LAS h16x8*)(pb + ks * 32); }
    WAIT_LGKM0();
    *(volatile LAS u32*)(sm + K1_FL + sf) = 0x01010101u;
#pragma unroll
    for (int ks = 0; ks < 8; ++ks) tacc = __builtin_amdgcn_mfma_f32_32x32x16_f16(fa[ks], fb[ks], tacc, 0, 0, 0);
    __builtin_amdgcn_s_setprio(HPRIO);
  }
  if (kw >= 6) { WAIT_VM(0); TAG_READY(raw, tag); }
  if (kw == 6) {
    __builtin_amdgcn_s_setprio(3);
    poll_flags(flags + FL_U + (pm * 16 + (lane & 15)) * FSP, tag);
    const __amdgpu_buffer_rsrc_t srs = __builtin_amdgcn_make_buffer_rsrc(RST, 0, L * 8, 0x00020000);
    u32x4 sv = __builtin_amdgcn_raw_buffer_load_b128(srs, (u32)((pm * 128 + 2 * lane) * 8), 0, 16);
    *(LAS u32x4*)(sm + G1_RS + lane * 16) = sv;
    poll_flags(flags + FL_C + min((128 * pn) / 56 + (lane & 3), 31) * FSP, tag);
    const __amdgpu_buffer_rsrc_t crs = __builtin_amdgcn_make_buffer_rsrc(RST + (WS_CS - WS_RINV), 0, FF * 8, 0x00020000);
    u32x4 cv = __builtin_amdgcn_raw_buffer_load_b128(crs, (u32)((pn * 128 + 2 * lane) * 8), 0, 16);
    *(LAS u32x4*)(sm + G1_CS + lane * 16) = cv;
    *(volatile LAS u32*)(sm + K1_FL + 184) = tag;
    WAIT_LGKM0();
    *(volatile LAS u32*)(sm + K1_FL + 176) = 0x01010101u;
    __builtin_amdgcn_s_setprio(0);
  }
  if (kw >= 6) { __builtin_amdgcn_s_setprio(3); poll_flags(flags + FL_U + ((kw - 6) * 128 + lane) * FSP, tag); poll_flags(flags + FL_U + ((kw - 6) * 128 + 64 + lane) * FSP, tag); __builtin_amdgcn_s_setprio(0); }
  lds_bar8s(sm, K1_FL + 56, kw);
  __builtin_amdgcn_s_setprio(3);
#pragma unroll
  for (int i = 0; i < 12; ++i) {
    const int a = (i & 3) + 8 * (i >> 2) + 4 * h;
    if (a < 21 && kw < 6) *(LAS float*)(sm + K2_PT + kw * 2688 + (a * 32 + r32) * 4) = tacc[i];
  }
  lds_bar8(sm, K1_FL + 64, kw);
  u32x4 uv;
  {
    __builtin_amdgcn_s_setprio(3);
    const __amdgpu_buffer_rsrc_t urs = __builtin_amdgcn_make_buffer_rsrc(UT, 0, 256 * UG, 0x00020000);
    uv = __builtin_amdgcn_raw_buffer_load_b128(urs, (u32)((4 * lane + wv) * UG + (d0 + ch) * 16), 0, 16);
  }
  {
    LAS char* pb0 = sm + K2_PT + ch * 4 * 2688 + t * 16;
    f32x4 v = {0.f, 0.f, 0.f, 0.f};
    if (t < 168) { v = *(LAS f32x4*)(pb0) + *(LAS f32x4*)(pb0 + 2688); if (!ch) v += *(LAS f32x4*)(pb0 + 2 * 2688) + *(LAS f32x4*)(pb0 + 3 * 2688); v *= F; }
    if (t < 184) *(LAS f32x4*)(sm + K2_T + ch * 2944 + t * 16) = v;
  }
  lds_bar8(sm, K1_FL + 72, kw);
  {
    float v[11];
#pragma unroll
    for (int e = 0; e < 11; ++e) {
      u32 w = e < 8 ? in.lut0[e >> 1] : in.lut1[(e - 8) >> 1];
      u32 off = (e & 1) ? (w >> 16) : (w & 0xffffu);
      v[e] = *(LAS float*)(sm + K2_T + off);
    }
    u32 ev[5], od[5];
#pragma unroll
    for (int i = 0; i < 5; ++i) { ev[i] = pk_f16((f32x2){v[2 * i], v[2 * i + 1]}); od[i] = pk_f16((f32x2){v[2 * i + 1], v[2 * i + 2]}); }
    LAS char* kt = sm + CV_K + ch * KT_BYTES + t * 16;
    *(LAS u32x4*)(kt + 3 * KT_COPY) = (u32x4){ev[0], ev[1], ev[2], ev[3]};
    *(LAS u32x4*)(kt + 2 * KT_COPY) = (u32x4){od[0], od[1], od[2], od[3]};
    *(LAS u32x4*)(kt + 1 * KT_COPY) = (u32x4){ev[1], ev[2], ev[3], ev[4]};
    *(LAS u32x4*)(kt + 0 * KT_COPY) = (u32x4){od[1], od[2], od[3], od[4]};
    if (lane < 32) *(LAS u32x4*)(sm + CV_U + ch * 6144 + wv * 1536 + lane * 16) = (u32x4){0u, 0u, 0u, 0u};
    if (t < 16) *(LAS u32x4*)(sm + CV_K + ch * KT_BYTES + (t >> 2) * KT_COPY + 4096 + (t & 3) * 16) = (u32x4){0u, 0u, 0u, 0u};
  }
  *(LAS u32x4*)(sm + CV_U + ch * 6144 + wv * 1536 + 512 + lane * 16) = uv;
  lds_bar8(sm, K1_FL + 80, kw);
  __builtin_amdgcn_s_setprio(CPRIO);
  f32x16 accA = {}, accB = {};
  {
    const u32 ka = smb + CV_K + ch * KT_BYTES + (r32 & 3) * KT_COPY + (2044 + 8 * h - 4 * (r32 >> 2)) * 2 - 7 * 64;
    const u32 ub = smb + CV_U + ch * 6144 + h * 1536 + (32 + r32) * 16 - 7 * 16;
    u32 kb[3], uu[3];
#pragma unroll
    for (int i = 0; i < 3; ++i) { const int sg = 3 * wv + i, g = sg >= 4, dl0 = 8 * (g ? sg - 4 : sg); kb[i] = ka - dl0 * 64; uu[i] = ub + (32 * g - dl0) * 16; }
    u32x2 a0[2][4], a1[2][4]; u32x4 bb[2][4];
#define CV_RD(n, buf, m) do { DSR64(a0[buf][m], kb[(n) >> 2], (7 - 2 * ((n) & 3) - ((m) >> 1)) * 64 + ((m) & 1) * 32); \
                              DSR64(a1[buf][m], kb[(n) >> 2], (7 - 2 * ((n) & 3) - ((m) >> 1)) * 64 + ((m) & 1) * 32 + 8); \
                              DSR128(bb[buf][m], uu[(n) >> 2], (7 - 2 * ((n) & 3) - ((m) >> 1)) * 16 + ((m) & 1) * 3072); } while (0)
#define CV_WAIT(buf, cnt) asm volatile("s_waitcnt lgkmcnt(" #cnt ")" : "+v"(a0[buf][0]), "+v"(a0[buf][1]), "+v"(a0[buf][2]), "+v"(a0[buf][3]), "+v"(a1[buf][0]), "+v"(a1[buf][1]), "+v"(a1[buf][2]), "+v"(a1[buf][3]), \
                                       "+v"(bb[buf][0]), "+v"(bb[buf][1]), "+v"(bb[buf][2]), "+v"(bb[buf][3]) :: "memory")
#define CV_MM(acc, buf, m) acc = __builtin_amdgcn_mfma_f32_32x32x16_f16(__builtin_bit_cast(h16x8, (u32x4){a0[buf][m].x, a0[buf][m].y, a1[buf][m].x, a1[buf][m].y}), __builtin_bit_cast(h16x8, bb[buf][m]), acc, 0, 0, 0)
#define CV_BATCH(n, acc) do { CV_WAIT((n) & 1, 0); \
      CV_MM(acc, (n) & 1, 0); __builtin_amdgcn_sched_barrier(0); if ((n) < 11) { CV_RD((n) + 1, ((n) + 1) & 1, 0); CV_RD((n) + 1, ((n) + 1) & 1, 1); CV_RD((n) + 1, ((n) + 1) & 1, 2); CV_RD((n) + 1, ((n) + 1) & 1, 3); } __builtin_amdgcn_sched_barrier(0); \
      CV_MM(acc, (n) & 1, 1); CV_MM(acc, (n) & 1, 2); CV_MM(acc, (n) & 1, 3); __builtin_amdgcn_sched_barrier(0); } while (0)
    CV_RD(0, 0, 0); CV_RD(0, 0, 1); CV_RD(0, 0, 2); CV_RD(0, 0, 3);
    CV_BATCH(0, accA); CV_BATCH(1, accA); CV_BATCH(2, accA); CV_BATCH(3, accA);
    CV_BATCH(4, accB); CV_BATCH(5, accB); CV_BATCH(6, accB); CV_BATCH(7, accB);
    CV_BATCH(8, accB); CV_BATCH(9, accB); CV_BATCH(10, accB); CV_BATCH(11, accB);
  }
  lds_bar8(sm, K1_FL + 96, kw);
  {
    const int slot = wv == 0 ? 0 : wv + 1;
    LAS char* pp = sm + CV_P + ch * 20480 + slot * 4096 + r32 * 128;
    if (wv == 1) {
#pragma unroll
      for (int g = 0; g < 4; ++g) *(LAS f32x4*)(pp - 4096 + (((2 * g + h) ^ (r32 & 7)) << 4)) = (f32x4){accA[4 * g], accA[4 * g + 1], accA[4 * g + 2], accA[4 * g + 3]};
    } else {
      accB += accA;
    }
#pragma unroll
    for (int g = 0; g < 4; ++g) *(LAS f32x4*)(pp + (((2 * g + h) ^ (r32 & 7)) << 4)) = (f32x4){accB[4 * g], accB[4 * g + 1], accB[4 * g + 2], accB[4 * g + 3]};
  }
  lds_bar8(sm, K1_FL + 104, kw);
  {
    const int a = t >> 2, bq = t & 3, g = a >> 5, ar = a & 31;
    LAS char* pp = sm + CV_P + ch * 20480 + (g ? 2 * 4096 : 0) + ar * 128;
    const u32 o0 = ((2 * bq) ^ (ar & 7)) << 4, o1 = ((2 * bq + 1) ^ (ar & 7)) << 4;
    f32x4 y0 = *(LAS f32x4*)(pp + o0) + *(LAS f32x4*)(pp + 4096 + o0);
    f32x4 y1 = *(LAS f32x4*)(pp + o1) + *(LAS f32x4*)(pp + 4096 + o1);
    if (g) { y0 += *(LAS f32x4*)(pp + 8192 + o0); y1 += *(LAS f32x4*)(pp + 8192 + o1); }
    h16x8 u = *(LAS h16x8*)(sm + CV_U + ch * 6144 + bq * 1536 + (32 + a) * 16);
    const float ds = __uint_as_float(in.dsk << 16);
    float r[8];
#pragma unroll
    for (int i = 0; i < 8; ++i) { float y = (i < 4 ? y0[i & 3] : y1[i & 3]) * invK; r[i] = rbf(y) + rbf((float)u[i] * ds); }
    u32x4 ov = {pk_bf16(r[0], r[1]), pk_bf16(r[2], r[3]), pk_bf16(r[4], r[5]), pk_bf16(r[6], r[7])};
    const __amdgpu_buffer_rsrc_t rs = __builtin_amdgcn_make_buffer_rsrc(ST, 0, D * LP * 2, 0x00020000);
    __builtin_amdgcn_s_setprio(3);
    __builtin_amdgcn_raw_buffer_store_b128(ov, rs, (u32)(((d0 + ch) * LP + 8 * t) * 2), 0, 16);
  }
  WAIT_VM(0);
  {
    u32 old = 0;
    if (lane == 0) old = __hip_atomic_fetch_add((LAS u32*)(sm + K1_FL + 88), 1u, __ATOMIC_RELAXED, __HIP_MEMORY_SCOPE_WORKGROUP);
    if (lane == 0 && old == 7) __hip_atomic_store((gu32*)(flags + FL_S + bid * FSP), tag, RLX_AGENT);
  }
  __builtin_amdgcn_s_setprio(0);
}

#define ROT2 ((7 * (idx & 3) / 2 + 2 * (idx >> 2)) % 14)
__global__ __launch_bounds__(768, 3) void p_kernel(const u16* x, const u16* logA, const u16* Aim, const u16* Cr, const u16* Ci, const u16* logdt,
                                                   const u16* Dskip, const u16* w1, const u16* w2, char* ws, u16* out) {
  extern __shared__ __attribute__((aligned(16))) char smem[];
  LAS char* sm = (LAS char*)smem;
  const int tid = threadIdx.x, lane = tid & 63;
  const int wave = __builtin_amdgcn_readfirstlane(tid >> 6);
  const int bid = (int)blockIdx.x >= 32 ? (int)blockIdx.x - 32 : (int)blockIdx.x + 224;
  const int xcd = bid & 7, idx = bid >> 3;
  const int pm = (xcd >> 1) * 4 + (idx & 3), pn = (xcd & 1) * 7 + (idx >> 2);
  const int pm2 = xcd * 4 + (idx & 3), pc = idx >> 2;
  const int rot1 = (2 * (idx & 3) + (idx >> 2)) % 7, rot2 = ROT2;
  u32* flags = (u32*)(ws + WS_FLAG);
  u32 tag = 0;
  if (tid < 64) *(LAS u32*)(sm + K1_FL + tid * 4) = 0u;
  WAIT_LGKM0(); CBAR();
  if (bid >= 224) {
    if (wave < 4) {
      const int e = bid - 224, g = (e >> 1) * 16 + 14 + (e & 1);
      u32x4 xrow = *(const u32x4*)(x + (size_t)(g * 8 + 2 * wave) * D + 8 * min(lane, 55));
      u32x4 xrow1 = *(const u32x4*)(x + (size_t)(g * 8 + 2 * wave + 1) * D + 8 * min(lane, 55));
      tag = epoch_load(flags) + 1u;
      ln_group_rows(sm, xrow, xrow1, lane, wave, g, ws + WS_UT, ws + WS_RINV);
      WAIT_VM(0);
      ln_publish(sm, lane, flags + FL_U + g * FSP, tag);
    }
    {
      const int e = bid - 224;
      LAS float* ps = (LAS float*)(sm + 65536);
      if (wave >= 4) {
        const int tt = tid - 256, c = tt % 7, rg = tt / 7;
        if (rg < 64) {
          u32x4 v[7];
#pragma unroll
          for (int i = 0; i < 7; ++i) v[i] = *(const u32x4*)(w1 + (size_t)(rg * 7 + i) * FF + 56 * e + 8 * c);
          float a8[8] = {0.f, 0.f, 0.f, 0.f, 0.f, 0.f, 0.f, 0.f};
#pragma unroll
          for (int i = 0; i < 7; ++i)
#pragma unroll
            for (int j = 0; j < 4; ++j) { a8[2 * j] += __uint_as_float(v[i][j] << 16); a8[2 * j + 1] += __uint_as_float(v[i][j] & 0xffff0000u); }
          *(LAS f32x4*)(ps + rg * 56 + 8 * c) = (f32x4){a8[0], a8[1], a8[2], a8[3]};
          *(LAS f32x4*)(ps + rg * 56 + 8 * c + 4) = (f32x4){a8[4], a8[5], a8[6], a8[7]};
        }
        lds_bar8(sm, K1_FL + 64, wave - 4);
        if (wave == 4) {
          const u32 tg = epoch_load(flags) + 1u;
          float c0 = 0.f;
          const int col = min(lane, 55);
#pragma unroll 8
          for (int r = 0; r < 64; ++r) c0 += ps[r * 56 + col];
          float p0 = rbf(c0), r1 = c0 - p0, p1 = rbf(r1), p2 = r1 - p1;
          const __amdgpu_buffer_rsrc_t crs = __builtin_amdgcn_make_buffer_rsrc(ws + WS_CS, 0, FF * 8, 0x00020000);
          if (lane < 56) __builtin_amdgcn_raw_buffer_store_b64((u32x2){pk_bf16(p0, p1), pk_bf16(p2, 0.f)}, crs, (u32)((56 * e + lane) * 8), 0, 16);
          WAIT_VM(0);
          if (lane == 0) __hip_atomic_store((gu32*)(flags + FL_C + e * FSP), tg, RLX_AGENT);
          if (e == 0) {
#pragma unroll 1
            for (int i = 0; i < 4; ++i) poll_flags(flags + FL_S + min(i * 64 + lane, 223) * FSP, tg);
            if (lane == 0) __hip_atomic_store((gu32*)flags, tg, RLX_AGENT);
          }
        }
      }
    }
    return;
  }
  if (wave < 4) {
    m_role(sm, wave, lane, pm, pn, pm2, pc, rot1, rot2, tag, x, w1, ws + WS_UT, ws + WS_RINV, ws + WS_H, w2, flags);
  } else {
    h_role(sm, wave - 4, lane, xcd * 28 + idx, pm, pn, pm2, rot1, tag, x, w1, logA, Aim, Cr, Ci, logdt, Dskip, ws + WS_UT, ws + WS_RINV, (u16*)(ws + WS_ST), flags);
  }
  const int e = tid - 256, row = (e >> 3) & 63, cq = e & 7;
  u32x4 xv; float rinv = 0.f, sval[8];
  if (wave >= 4) {
    xv = *(const u32x4*)(x + (size_t)(pm2 * 64 + row) * D + pc * 64 + 8 * cq);
    const __amdgpu_buffer_rsrc_t srs = __builtin_amdgcn_make_buffer_rsrc(ws + WS_RINV, 0, L * 8, 0x00020000);
    rinv = __uint_as_float(__builtin_amdgcn_raw_buffer_load_b32(srs, (u32)((pm2 * 64 + row) * 8 + 4), 0, 16));
    if (wave == 4) poll_flags(flags + FL_S + (pc * 32 + (lane & 31)) * FSP, tag);
    lds_bar8s(sm, K1_FL + 120, wave - 4);
    const __amdgpu_buffer_rsrc_t rs = __builtin_amdgcn_make_buffer_rsrc(ws + WS_ST, 0, D * LP * 2, 0x00020000);
    u32x4 sv = __builtin_amdgcn_raw_buffer_load_b128(rs, (u32)(((pc * 64 + (e >> 3)) * LP + pm2 * 64 + 8 * (e & 7)) * 2), 0, 16);
    *(LAS u32x4*)(sm + K2_S + (e >> 3) * 144 + (e & 7) * 16) = sv;
    lds_bar8(sm, K1_FL + 112, wave - 4);
#pragma unroll
    for (int i = 0; i < 8; ++i) sval[i] = bf2f(*(LAS u16*)(sm + K2_S + (8 * cq + i) * 144 + row * 2));
  }
  WAIT_LGKM0(); CBAR();
  if (wave >= 4) {
    f32x4 z0 = {0.f, 0.f, 0.f, 0.f}, z1 = {0.f, 0.f, 0.f, 0.f};
    const u32 ro = P2_R + row * 272 + cq * 32, rstep = 24576;
#pragma unroll
    for (int w = 0; w < 4; ++w) {
      z0 += *(LAS f32x4*)(sm + ro + w * rstep);
      z1 += *(LAS f32x4*)(sm + ro + w * rstep + 16);
    }
    float r[8];
#pragma unroll
    for (int i = 0; i < 8; ++i) {
      float o = rbf((i < 4 ? z0[i & 3] : z1[i & 3]) * rinv);
      float g = rbf(o * __builtin_amdgcn_rcpf(1.f + __builtin_amdgcn_exp2f(-1.4426950408889634f * o)));
      float xx = (i & 1) ? __uint_as_float(xv[i >> 1] & 0xffff0000u) : __uint_as_float(xv[i >> 1] << 16);
      r[i] = xx + rbf(sval[i] * g);
    }
    *(u32x4*)(out + (size_t)(pm2 * 64 + row) * D + pc * 64 + 8 * cq) = (u32x4){pk_bf16(r[0], r[1]), pk_bf16(r[2], r[3]), pk_bf16(r[4], r[5]), pk_bf16(r[6], r[7])};
  }
}
}

#define LDSZ(x) (x)
extern "C" void kernel_launch(void* const* d_in, const int* in_sizes, int n_in,
                              void* d_out, int out_size, void* d_ws, size_t ws_size,
                              hipStream_t stream) {
  (void)hipFuncSetAttribute((const void*)p_kernel, hipFuncAttributeMaxDynamicSharedMemorySize, LDSZ(P_LDS));
  p_kernel<<<256, 768, LDSZ(P_LDS), stream>>>((const u16*)d_in[0], (const u16*)d_in[1], (const u16*)d_in[2], (const u16*)d_in[3], (const u16*)d_in[4], (const u16*)d_in[5],
                                              (const u16*)d_in[6], (const u16*)d_in[8], (const u16*)d_in[9], (char*)d_ws, (u16*)d_out);
}
```
